```python
import torch

def _softmax(x, axis=-1):
    return torch.softmax(x, dim=axis)
def _gelu(x):
    return torch.nn.functional.gelu(x, approximate="tanh")
def _silu(x):
    return torch.nn.functional.silu(x)
def _sigmoid(x):
    return torch.sigmoid(x)
def _layernorm(x, axis=-1, eps=1e-5):
    m = x.mean(dim=axis, keepdim=True)
    v = x.var(dim=axis, keepdim=True, unbiased=False)
    return (x - m) / torch.sqrt(v + eps)
def _scatter_add(tgt, idx, src, axis=0):
    return tgt.clone().index_add_(axis, idx, src)
def _segment_sum(src, seg, n):
    out = torch.zeros((n,) + src.shape[1:], dtype=src.dtype)
    return out.index_add_(0, seg, src)
def _pad(x, pw):
    flat = tuple(v for pair in reversed(pw) for v in pair)
    return torch.nn.functional.pad(x, flat)

kernel_name = 'gated_mix__linear_attn_block__mixer_s_mlp__prompt'

def setup_inputs():

    _w = (lambda a: (lambda t: t.to(torch.bfloat16) if t.is_floating_point() else t)(torch.as_tensor(a)))
    def _A_setup():
        B, S, D = 1, 2048, 1024
        return {
            "x": _w((lambda *s: torch.randn(*s, dtype=torch.bfloat16) * 0.05)(B, S, D)),
            "wqkv": _w((lambda *s: torch.randn(*s, dtype=torch.bfloat16) * 3.2)(D, 3 * D)),
            "wo": _w((lambda *s: torch.randn(*s, dtype=torch.bfloat16) * 1.6)(D, D)),
        }
    _A_in = _A_setup()

    def _B_setup():
        B, S, D, F = 1, 2048, 1024, 3584
        return {
            "x":  _w((lambda *s: torch.randn(*s, dtype=torch.bfloat16) * 0.05)(B, S, D)),
            "w1": _w((lambda *s: torch.randn(*s, dtype=torch.bfloat16) * 3.2)(D, D) * (D ** -0.5)),
            "w2": _w((lambda *s: torch.randn(*s, dtype=torch.bfloat16) * 3.2)(D, D) * (D ** -0.5)),
            "w3": _w((lambda *s: torch.randn(*s, dtype=torch.bfloat16) * 3.2)(D, F) * (D ** -0.5)),
            "w4": _w((lambda *s: torch.randn(*s, dtype=torch.bfloat16) * 1.6)(F, D) * (F ** -0.5)),
        }
    _B_in = _B_setup()

    return {
        "A_x": _A_in["x"],
        "A_wqkv": _A_in["wqkv"],
        "A_wo": _A_in["wo"],
        "B_x": _B_in["x"],
        "B_w1": _B_in["w1"],
        "B_w2": _B_in["w2"],
        "B_w3": _B_in["w3"],
        "B_w4": _B_in["w4"],
        "gate_w": _w((lambda *s: torch.randn(*s, dtype=torch.bfloat16) * 0.05)(1024) * 16.0),
    }

def reference(A_x, A_wqkv, A_wo, B_x, B_w1, B_w2, B_w3, B_w4, gate_w):
    def _A_ref(x, wqkv, wo):
        B, S, D = x.shape
        H = 32; Dh = D // H
        qkv = (lambda x, *d: x.permute(*d))((x @ wqkv).reshape(B, S, 3, H, Dh), 2, 0, 3, 1, 4)
        q, k, v = qkv[0], qkv[1], qkv[2]
        phi = lambda t: torch.where(t > 0, t + 1, torch.exp(t)) + 1e-6
        pq, pk = phi(q), phi(k)
        kv = torch.einsum("bhsd,bhse->bhde", pk, v)
        z = torch.einsum("bhsd->bhd", pk)
        num = torch.einsum("bhqd,bhde->bhqe", pq, kv)
        den = torch.einsum("bhqd,bhd->bhq", pq, z)[..., None] + 1e-6
        o = num / den
        return (lambda x, *d: x.permute(*d))(o, 0, 2, 1, 3).reshape(B, S, D) @ wo

    def _B_ref(x, w1, w2, w3, w4):
        B, S, D = x.shape
        h = _gelu(_layernorm(x, axis=-1) @ w1)
        c = D // 4
        g0 = _pad(h[:, :, 0*c:1*c], ((0, 0), (1, 0), (0, 0)))[:, :S, :]
        g1 = _pad(h[:, :, 1*c:2*c], ((0, 0), (0, 1), (0, 0)))[:, 1:, :]
        g2 = _pad(h[:, :, 2*c:3*c], ((0, 0), (2, 0), (0, 0)))[:, :S, :]
        g3 = _pad(h[:, :, 3*c:   ], ((0, 0), (0, 2), (0, 0)))[:, 2:, :]
        hs = torch.concatenate([g0, g1, g2, g3], axis=-1) @ w2
        x1 = x + hs
        h2 = _gelu(_layernorm(x1, axis=-1) @ w3) @ w4
        return x1 + h2

    x = A_x
    g = _sigmoid((x * gate_w).sum(-1, keepdims=True))
    a = _A_ref(x, A_wqkv, A_wo)
    b = _B_ref(x, B_w1, B_w2, B_w3, B_w4)
    return g * a + (1 - g) * b
```

```cpp
#include <hip/hip_runtime.h>
#include <hip/hip_bf16.h>
#include <stdint.h>

namespace {

typedef unsigned short u16;
typedef unsigned int u32;
typedef short s16x4 __attribute__((ext_vector_type(4)));
typedef short s16x8 __attribute__((ext_vector_type(8)));
typedef __bf16 bf16x8 __attribute__((ext_vector_type(8)));
typedef __bf16 bf16x2 __attribute__((ext_vector_type(2)));
typedef float f32x2 __attribute__((ext_vector_type(2)));
typedef float f32x4 __attribute__((ext_vector_type(4)));
typedef u32 u32x2 __attribute__((ext_vector_type(2)));
typedef u32 u32x4 __attribute__((ext_vector_type(4)));
#define LAS __attribute__((address_space(3)))
#define DI __device__ __forceinline__

constexpr int SEQ = 2048, DM = 1024, FF = 3584, KPG = DM + FF;

constexpr size_t OFF_H   = 0;
constexpr size_t OFF_X1  = OFF_H + (size_t)(SEQ + 4) * DM * 2;
constexpr size_t OFF_PG  = OFF_X1 + (size_t)SEQ * DM * 2;
constexpr size_t OFF_M   = OFF_PG + (size_t)SEQ * KPG * 2;
constexpr size_t OFF_KVP = OFF_M + (size_t)DM * DM * 2;
constexpr size_t OFF_ZP  = OFF_KVP + (size_t)8 * 32 * 1024 * 4;
constexpr size_t OFF_G   = OFF_ZP + (size_t)8 * 32 * 32 * 4;
constexpr size_t OFF_ST  = OFF_G + (size_t)SEQ * 4;
constexpr size_t OFF_CS3 = OFF_ST + (size_t)SEQ * 16 * 8;

DI u32 pk2(float a, float b) { bf16x2 v = __builtin_convertvector((f32x2){a, b}, bf16x2); return __builtin_bit_cast(u32, v); }
DI float bf_lo(u32 u) { return __uint_as_float(u << 16); }
DI float bf_hi(u32 u) { return __uint_as_float(u & 0xffff0000u); }
DI bf16x8 lds_b128(u32 a) { return *(const LAS bf16x8*)a; }
DI s16x4 lds_tr(u32 a) { return __builtin_amdgcn_ds_read_tr16_b64_v4i16((LAS s16x4*)a); }
DI bf16x8 cat8(s16x4 a, s16x4 b) { return __builtin_bit_cast(bf16x8, __builtin_shufflevector(a, b, 0, 1, 2, 3, 4, 5, 6, 7)); }
DI f32x4 mfma(bf16x8 a, bf16x8 b, f32x4 c) { return __builtin_amdgcn_mfma_f32_16x16x32_bf16(a, b, c, 0, 0, 0); }
DI void bar() { asm volatile("" ::: "memory"); __builtin_amdgcn_s_barrier(); asm volatile("" ::: "memory"); }
DI void wait_lgkm0() { asm volatile("" ::: "memory"); __builtin_amdgcn_s_waitcnt(0xC07F); asm volatile("" ::: "memory"); }
template <int N> DI void wait_vm() { asm volatile("s_waitcnt vmcnt(%0)" ::"n"(N) : "memory"); }
#define SCHED() __builtin_amdgcn_sched_barrier(0)

DI void glds16(u32 lds, u32 voff, const void* gbase) {
  u32 keep;
  asm volatile("s_mov_b32 %0, m0\n\ts_mov_b32 m0, %1\n\ts_nop 3\n\tglobal_load_lds_dwordx4 %2, %3\n\ts_mov_b32 m0, %0"
               : "=&s"(keep) : "s"(lds), "v"(voff), "s"(gbase) : "memory");
}

DI float grp_sum(float v) {
  u32 u = __float_as_uint(v);
  auto r = __builtin_amdgcn_permlane16_swap(u, u, false, false);
  float s = __uint_as_float(r[0]) + __uint_as_float(r[1]);
  u = __float_as_uint(s);
  auto q = __builtin_amdgcn_permlane32_swap(u, u, false, false);
  return __uint_as_float(q[0]) + __uint_as_float(q[1]);
}

DI float gelu_tanh(float x) {
  const float c0 = -2.0f * 0.7978845608028654f * 1.4426950408889634f, c1 = c0 * 0.044715f;
  const float e = __builtin_amdgcn_exp2f(x * (c0 + c1 * x * x));
  return x * __builtin_amdgcn_rcpf(1.0f + e);
}
DI float phi(float t) { return (t > 0.0f ? t + 1.0f : __builtin_amdgcn_exp2f(t * 1.4426950408889634f)) + 1e-6f; }

DI void swap_pair_f32(f32x4& a, f32x4& b) {
#pragma unroll
  for (int r = 0; r < 4; ++r) {
    auto s = __builtin_amdgcn_permlane16_swap(__float_as_uint(a[r]), __float_as_uint(b[r]), false, false);
    a[r] = __uint_as_float(s[0]); b[r] = __uint_as_float(s[1]);
  }
}
DI u32x4 pack_pair(const f32x4& a, const f32x4& b) {
  auto s0 = __builtin_amdgcn_permlane16_swap(pk2(a[0], a[1]), pk2(b[0], b[1]), false, false);
  auto s1 = __builtin_amdgcn_permlane16_swap(pk2(a[2], a[3]), pk2(b[2], b[3]), false, false);
  return (u32x4){s0[0], s1[0], s0[1], s1[1]};
}

template <int P, int R> DI void wait_rem(int rem) {
  if constexpr (R == 0) { wait_vm<0>(); }
  else { if (rem >= R) wait_vm<P * R>(); else wait_rem<P, R - 1>(rem); }
}

template <int NPX, int NPW, int S, int XBYTES, int STAGE, class Addr>
DI void loader_loop(int NT, int lw, const u32 (&vx)[NPX], const u32 (&vw)[NPW], bool wvalid0, bool wvalid1, const Addr& addr) {
  constexpr int P = NPX + NPW;
  auto issue = [&](int t, int st) {
    const char *xb, *wb;
    addr(t, xb, wb);
    const u32 base = (u32)st * STAGE;
#pragma unroll
    for (int j = 0; j < NPX; ++j) glds16(base + (u32)(lw * NPX + j) * 1024u, vx[j], xb);
#pragma unroll
    for (int j = 0; j < NPW; ++j) {
      if ((j & 2) ? wvalid1 : wvalid0) glds16(base + XBYTES + (u32)(lw * NPW + j) * 1024u, vw[j], wb);
    }
  };
#pragma unroll
  for (int t = 0; t < S; ++t) issue(t, t);
  wait_vm<P*(S - 1)>();
  bar();
  int st = 0;
  for (int t = 1; t < NT; ++t) {
    wait_rem<P, S - 2>(NT - 1 - t);
    bar();
    if (t - 1 + S < NT) issue(t - 1 + S, st);
    st = (st + 1 == S) ? 0 : st + 1;
  }
}

struct NoExtra {
  template <int KS, int WM_T, int WN_T> DI void step(const bf16x8 (&)[WM_T], const bf16x8 (&)[WN_T]) {}
};

template <int WM_T, int WN_T, bool ORIENT_T>
DI void mma_row(f32x4 (&acc)[WN_T], const bf16x8& x, const bf16x8 (&w)[WN_T]) {
#pragma unroll
  for (int n = 0; n < WN_T; ++n) acc[n] = ORIENT_T ? mfma(w[n], x, acc[n]) : mfma(x, w[n], acc[n]);
}

template <int WN_T, int WROWB>
DI void read_w(bf16x8 (&w)[WN_T], const u32 (&wa)[WN_T], u32 off) {
#pragma unroll
  for (int n = 0; n < WN_T; ++n) w[n] = cat8(lds_tr(wa[n] + off), lds_tr(wa[n] + off + 4 * WROWB));
}

template <int WM_T, int WN_T, int WROWB>
DI void rd_units(int lo, int hi, bf16x8 (&X)[WM_T], bf16x8 (&W)[WN_T], u32 xa, const u32 (&wa)[WN_T], u32 woff) {
#pragma unroll
  for (int u = 0; u < WN_T + WM_T; ++u) {
    if (u >= lo && u < hi) {
      if (u < WN_T) W[u] = cat8(lds_tr(wa[u] + woff), lds_tr(wa[u] + woff + 4 * WROWB));
      else X[u - WN_T] = lds_b128(xa + (u - WN_T) * 2048);
    }
  }
}

template <int WM_T, int WN_T, int WROWB, bool ORIENT_T, int LAG, bool MORE, class Extra>
DI void k_tile(f32x4 (&acc)[WM_T][WN_T], bf16x8 (&X0)[WM_T], bf16x8 (&W0)[WN_T], bf16x8 (&X1)[WM_T], bf16x8 (&W1)[WN_T], u32 cur, u32 nxt,
               u32 xa0, u32 xa1, const u32 (&wa)[WN_T], Extra& ex) {
  constexpr int U = WN_T + WM_T;
  constexpr int Q0 = (U + WM_T - 2) / (WM_T - 1);
  constexpr int Q1 = (U + WM_T - LAG - 1) / (WM_T - LAG);
#pragma unroll
  for (int m = 0; m < WM_T; ++m) {
    mma_row<WM_T, WN_T, ORIENT_T>(acc[m], X0[m], W0);
    if (m == 0) ex.template step<0, WM_T, WN_T>(X0, W0);
    rd_units<WM_T, WN_T, WROWB>(m * Q0, (m + 1) * Q0, X1, W1, xa1 + cur, wa, cur + 32 * WROWB);
    SCHED();
  }
#pragma unroll
  for (int m = 0; m < WM_T; ++m) {
    if (m == LAG && MORE) {
      wait_lgkm0();
      bar();
      rd_units<WM_T, WN_T, WROWB>(0, Q1, X0, W0, xa0 + nxt, wa, nxt);
      SCHED();
    }
    mma_row<WM_T, WN_T, ORIENT_T>(acc[m], X1[m], W1);
    if (m == WM_T - 1) ex.template step<1, WM_T, WN_T>(X1, W1);
    if (MORE && m >= LAG) rd_units<WM_T, WN_T, WROWB>((m - LAG + 1) * Q1, (m - LAG + 2) * Q1, X0, W0, xa0 + nxt, wa, nxt);
    SCHED();
  }
}

template <int WM_T, int WN_T, int WROWB, int S, int STAGE, bool ORIENT_T, int LAG, class Extra>
DI void compute_loop(f32x4 (&acc)[WM_T][WN_T], int NT, u32 xa0, u32 xa1, const u32 (&wa)[WN_T], Extra& ex) {
  bf16x8 X0[WM_T], X1[WM_T], W0[WN_T], W1[WN_T];
  rd_units<WM_T, WN_T, WROWB>(0, WM_T + WN_T, X0, W0, xa0, wa, 0);
  u32 cur = 0;
  for (int t = 0; t < NT - 1; ++t) {
    const u32 nxt = (cur + STAGE == (u32)S * STAGE) ? 0u : cur + STAGE;
    k_tile<WM_T, WN_T, WROWB, ORIENT_T, LAG, true>(acc, X0, W0, X1, W1, cur, nxt, xa0, xa1, wa, ex);
    cur = nxt;
  }
  k_tile<WM_T, WN_T, WROWB, ORIENT_T, LAG, false>(acc, X0, W0, X1, W1, cur, 0u, xa0, xa1, wa, ex);
}

DI void x_addrs(int lane, int row0, u32& xa0, u32& xa1) {
  const u32 r = (u32)(row0 + (lane & 15)) * 128u, g = lane >> 4, f = lane & 7;
  xa0 = r + ((g ^ f) << 4);
  xa1 = r + (((4 + g) ^ f) << 4);
}
template <int WN_T, int WROWB> DI void w_addrs(int lane, int nt0, u32 wbase, u32 (&wa)[WN_T]) {
  const u32 g = lane >> 4, q = (lane & 15) >> 2, p = lane & 3;
  const u32 xm = (WROWB == 256) ? ((q | ((g & 1) << 2)) << 1) : ((((q >> 1) & 1) | ((g & 1) << 1)) << 1);
#pragma unroll
  for (int n = 0; n < WN_T; ++n) wa[n] = wbase + (8 * g + q) * WROWB + (((u32)(2 * (nt0 + n)) + (p >> 1)) ^ xm) * 16u + (p & 1) * 8u;
}
template <int NPX> DI void x_voffs(int lane, int lw, int ld, u32 (&vx)[NPX]) {
#pragma unroll
  for (int j = 0; j < NPX; ++j) vx[j] = (u32)((8 * (lw * NPX + j) + (lane >> 3)) * ld) * 2u + (u32)(((lane & 7) ^ (lane >> 3)) << 4);
}
DI void w256_src(int lane, int piece, int& k, int& c) {
  k = 4 * piece + (lane >> 4);
  c = (lane & 15) ^ (((k & 3) | (((k >> 3) & 1) << 2)) << 1);
}
DI void w128_src(int lane, int piece, int& k, int& c) {
  k = 8 * piece + (lane >> 3);
  c = (lane & 7) ^ ((((k >> 1) & 1) | (((k >> 3) & 1) << 1)) << 1);
}

DI bf16x8 ones8() { const u32 o = 0x3f803f80u; return __builtin_bit_cast(bf16x8, (u32x4){o, o, o, o}); }

constexpr int K1_LDS = 3 * 49152;
constexpr u32 T1_GW = 131072, T1_ONE = T1_GW + 2048, T1_SUM = T1_ONE + 64, T1_SSQ = T1_SUM + 512, T1_CS = T1_SSQ + 512;

template <int WM, int WN> struct StatExtra {
  f32x4 (&racc)[4]; f32x4 (&cacc)[4];
  u32 sa, sstep;
  template <int KS, int WM_T, int WN_T> DI void step(const bf16x8 (&X)[WM_T], const bf16x8 (&W)[WN_T]) {
    if (WN == 0) {
#pragma unroll
      for (int m = 0; m < 4; ++m) racc[m] = mfma(X[m], X[m], racc[m]);
    } else {
      const bf16x8 a = lds_b128(sa);
      sa += sstep;
#pragma unroll
      for (int m = 0; m < 4; ++m) racc[m] = mfma(a, X[m], racc[m]);
    }
    if (WM == KS) {
      const bf16x8 o = ones8();
#pragma unroll
      for (int n = 0; n < 4; ++n) cacc[n] = mfma(W[n], o, cacc[n]);
    }
  }
};
template <int WM, int WN>
DI void w1_loop(f32x4 (&acc)[4][4], f32x4 (&racc)[4], f32x4 (&cacc)[4], u32 sa, u32 sstep, u32 xa0, u32 xa1, const u32 (&wa)[4]) {
  StatExtra<WM, WN> ex{racc, cacc, sa, sstep};
  compute_loop<4, 4, 256, 4, 32768, true, 1>(acc, 16, xa0, xa1, wa, ex);
}

__global__ __launch_bounds__(512, 2) void k1_kernel(const u16* __restrict__ x, const u16* __restrict__ wqkv, const u16* __restrict__ w1,
                                                    const u16* __restrict__ w3, const u16* __restrict__ gw, char* __restrict__ ws) {
  extern __shared__ __attribute__((aligned(16))) char smem[];
  const int b = blockIdx.x, xcd = b & 7, idx = b >> 3;
  const int wave = __builtin_amdgcn_readfirstlane(threadIdx.x >> 6), lane = threadIdx.x & 63;
  const int g = lane >> 4, l15 = lane & 15;
  if (idx < 16) {
    const int rp = (xcd >> 1) * 2 + (idx >> 3), hp = (xcd & 1) * 8 + (idx & 7);
    if (wave >= 4) {
      const int lw = wave - 4;
      u32 vx[8], vw[4];
      x_voffs<8>(lane, lw, DM, vx);
#pragma unroll
      for (int j = 0; j < 4; ++j) {
        int k, c; w256_src(lane, lw * 4 + j, k, c);
        vw[j] = (u32)(k * 3072 + 1024 * (1 + ((c >> 2) & 1)) + 32 * (2 * hp + (c >> 3)) + 8 * (c & 3)) * 2u;
      }
      const char* xb0 = (const char*)(x + (size_t)rp * 256 * DM);
      const char* wb0 = (const char*)wqkv;
      auto addr = [&](int t, const char*& xb, const char*& wb) { xb = xb0 + t * 128; wb = wb0 + (size_t)t * (64 * 3072 * 2); };
      loader_loop<8, 4, 3, 32768, 49152>(16, lw, vx, vw, true, true, addr);
      return;
    }
    const int wm = wave >> 1, wn = wave & 1;
    u32 xa0, xa1, wa[4];
    x_addrs(lane, wm * 128, xa0, xa1);
    w_addrs<4, 256>(lane, wn * 4, 32768, wa);
    f32x4 acc[8][4];
#pragma unroll
    for (int m = 0; m < 8; ++m)
#pragma unroll
      for (int n = 0; n < 4; ++n) acc[m][n] = (f32x4){0.f, 0.f, 0.f, 0.f};
    NoExtra ex;
    wait_lgkm0();
    bar();
    compute_loop<8, 4, 256, 3, 49152, false, 1>(acc, 16, xa0, xa1, wa, ex);
    f32x4 kv[2][2], z[2];
#pragma unroll
    for (int i = 0; i < 2; ++i) { z[i] = (f32x4){0.f, 0.f, 0.f, 0.f}; kv[0][i] = z[i]; kv[1][i] = z[i]; }
    const bf16x8 one = ones8();
#pragma unroll
    for (int mp = 0; mp < 4; ++mp) {
      bf16x8 pkf[2], vf[2];
#pragma unroll
      for (int i = 0; i < 2; ++i) {
        const f32x4 a = acc[2 * mp][i], c = acc[2 * mp + 1][i], d = acc[2 * mp][2 + i], e = acc[2 * mp + 1][2 + i];
        pkf[i] = __builtin_bit_cast(bf16x8, (u32x4){pk2(phi(a[0]), phi(a[1])), pk2(phi(a[2]), phi(a[3])), pk2(phi(c[0]), phi(c[1])), pk2(phi(c[2]), phi(c[3]))});
        vf[i] = __builtin_bit_cast(bf16x8, (u32x4){pk2(d[0], d[1]), pk2(d[2], d[3]), pk2(e[0], e[1]), pk2(e[2], e[3])});
      }
#pragma unroll
      for (int i = 0; i < 2; ++i) {
        kv[0][i] = mfma(vf[0], pkf[i], kv[0][i]);
        kv[1][i] = mfma(vf[1], pkf[i], kv[1][i]);
        z[i] = mfma(one, pkf[i], z[i]);
      }
    }
    wait_lgkm0();
    bar();
    LAS f32x4* red = (LAS f32x4*)(u32)(wn * 5 * 1024 + lane * 16);
    if (wm == 1) {
      red[0] = kv[0][0]; red[64] = kv[0][1]; red[128] = kv[1][0]; red[192] = kv[1][1];
      red[256] = (f32x4){z[0][0], z[1][0], 0.f, 0.f};
    }
    wait_lgkm0();
    bar();
    if (wm == 0) {
      const int head = 2 * hp + wn;
      float* kvp = (float*)(ws + OFF_KVP) + (size_t)(rp * 32 + head) * 1024;
#pragma unroll
      for (int j = 0; j < 2; ++j)
#pragma unroll
        for (int i = 0; i < 2; ++i) *(f32x4*)(kvp + (16 * i + l15) * 32 + 16 * j + 4 * g) = kv[j][i] + red[(2 * j + i) * 64];
      const f32x4 zo = red[256];
      if (g == 0) {
        float* zp = (float*)(ws + OFF_ZP) + (rp * 32 + head) * 32;
        zp[l15] = z[0][0] + zo[0];
        zp[16 + l15] = z[1][0] + zo[1];
      }
    }
    return;
  }
  const int i2 = idx - 16;
  const int rp = (xcd >> 1) * 4 + (i2 >> 2), cp = (xcd & 1) * 4 + (i2 & 3);
  if (wave >= 4) {
    const int lw = wave - 4;
    u32 vx[4], vw[4];
    x_voffs<4>(lane, lw, DM, vx);
#pragma unroll
    for (int j = 0; j < 4; ++j) {
      int k, c; w256_src(lane, lw * 4 + j, k, c);
      vw[j] = (u32)(k * DM + cp * 128 + 8 * c) * 2u;
    }
    const char* xb0 = (const char*)(x + (size_t)rp * 128 * DM);
    const char* wb0 = (const char*)w1;
    auto addr = [&](int t, const char*& xb, const char*& wb) { xb = xb0 + t * 128; wb = wb0 + (size_t)t * (64 * DM * 2); };
    loader_loop<4, 4, 4, 16384, 32768>(16, lw, vx, vw, true, true, addr);
    const int wb = xcd * 16 + i2;
    if (wb < FF / 32) {
      const u16* src = w3 + (size_t)(256 * lw + (lane >> 2)) * FF + wb * 32 + (lane & 3) * 8;
      u32x4 v[16];
#pragma unroll
      for (int i = 0; i < 16; ++i) v[i] = *(const u32x4*)(src + (size_t)(16 * i) * FF);
      float s[8];
#pragma unroll
      for (int e = 0; e < 8; ++e) s[e] = 0.f;
#pragma unroll
      for (int i = 0; i < 16; ++i)
#pragma unroll
        for (int e = 0; e < 4; ++e) { s[2 * e] += bf_lo(v[i][e]); s[2 * e + 1] += bf_hi(v[i][e]); }
#pragma unroll
      for (int e = 0; e < 8; ++e) {
        s[e] += __shfl_xor(s[e], 4); s[e] += __shfl_xor(s[e], 8);
        s[e] = grp_sum(s[e]);
      }
      if (lane < 4) {
        float* dst = (float*)(ws + OFF_CS3) + lw * FF + wb * 32 + lane * 8;
        *(f32x4*)dst = (f32x4){s[0], s[1], s[2], s[3]};
        *(f32x4*)(dst + 4) = (f32x4){s[4], s[5], s[6], s[7]};
      }
    } else if (wb == 127) {
      const int row = (lw < 2) ? lw : SEQ + lw;
      u32x4* dst = (u32x4*)(ws + OFF_H + (size_t)row * DM * 2);
      dst[lane] = (u32x4){0u, 0u, 0u, 0u};
      dst[64 + lane] = (u32x4){0u, 0u, 0u, 0u};
    }
    return;
  }
  const int wm = wave >> 1, wn = wave & 1;
  {
    const int tid = threadIdx.x;
    if (tid < 128) *(LAS u32x4*)(T1_GW + tid * 16) = *(const u32x4*)(gw + tid * 8);
    else if (tid == 128) *(LAS u32x4*)T1_ONE = (u32x4){0x3f803f80u, 0x3f803f80u, 0x3f803f80u, 0x3f803f80u};
  }
  u32 xa0, xa1, wa[4];
  x_addrs(lane, wm * 64, xa0, xa1);
  w_addrs<4, 256>(lane, wn * 4, 16384, wa);
  f32x4 acc[4][4], racc[4], cacc[4];
#pragma unroll
  for (int m = 0; m < 4; ++m) {
    racc[m] = (f32x4){0.f, 0.f, 0.f, 0.f}; cacc[m] = racc[m];
#pragma unroll
    for (int n = 0; n < 4; ++n) acc[m][n] = racc[m];
  }
  const u32 sa = (l15 == 1) ? T1_GW + g * 16 : T1_ONE, sstep = (l15 == 1) ? 64u : 0u;
  wait_lgkm0();
  bar();
  if (wave == 0) w1_loop<0, 0>(acc, racc, cacc, sa, sstep, xa0, xa1, wa);
  else if (wave == 1) w1_loop<0, 1>(acc, racc, cacc, sa, sstep, xa0, xa1, wa);
  else if (wave == 2) w1_loop<1, 0>(acc, racc, cacc, sa, sstep, xa0, xa1, wa);
  else w1_loop<1, 1>(acc, racc, cacc, sa, sstep, xa0, xa1, wa);
  LAS float* tsum = (LAS float*)T1_SUM; LAS float* tssq = (LAS float*)T1_SSQ; LAS float* tcs = (LAS float*)T1_CS;
  if (wn == 0) {
    if ((l15 >> 2) == g) {
#pragma unroll
      for (int m = 0; m < 4; ++m) {
        const f32x4 v = racc[m];
        const int r = lane & 3;
        tssq[wm * 64 + m * 16 + l15] = r == 0 ? v[0] : r == 1 ? v[1] : r == 2 ? v[2] : v[3];
      }
    }
  } else if (g == 0) {
    float* gate = (float*)(ws + OFF_G);
#pragma unroll
    for (int m = 0; m < 4; ++m) {
      tsum[wm * 64 + m * 16 + l15] = racc[m][0];
      if (cp == 0) gate[rp * 128 + wm * 64 + m * 16 + l15] = __builtin_amdgcn_rcpf(1.0f + __builtin_amdgcn_exp2f(-1.4426950408889634f * racc[m][1]));
    }
  }
  if (l15 == 0) {
#pragma unroll
    for (int n = 0; n < 4; ++n) *(LAS f32x4*)(tcs + wm * 128 + wn * 64 + n * 16 + 4 * g) = cacc[n];
  }
  wait_lgkm0();
  bar();
  f32x4 cs[4];
#pragma unroll
  for (int n = 0; n < 4; ++n) cs[n] = *(LAS f32x4*)(tcs + wn * 64 + n * 16 + 4 * g) + *(LAS f32x4*)(tcs + 128 + wn * 64 + n * 16 + 4 * g);
  u16* h = (u16*)(ws + OFF_H) + (size_t)(2 + rp * 128 + wm * 64 + l15) * DM + cp * 128 + wn * 64 + 16 * (g & 1) + 8 * (g >> 1);
#pragma unroll
  for (int m = 0; m < 4; ++m) {
    const float mean = tsum[wm * 64 + m * 16 + l15] * (1.0f / DM);
    const float var = fmaxf(tssq[wm * 64 + m * 16 + l15] * (1.0f / DM) - mean * mean, 0.0f);
    const float rstd = __builtin_amdgcn_rsqf(var + 1e-5f), mr = mean * rstd;
#pragma unroll
    for (int n = 0; n < 4; ++n)
#pragma unroll
      for (int r = 0; r < 4; ++r) acc[m][n][r] = gelu_tanh(acc[m][n][r] * rstd - mr * cs[n][r]);
    *(u32x4*)(h + (size_t)m * 16 * DM) = pack_pair(acc[m][0], acc[m][1]);
    *(u32x4*)(h + (size_t)m * 16 * DM + 32) = pack_pair(acc[m][2], acc[m][3]);
  }
}

constexpr u32 T2_WO = 131072, T2_KV = T2_WO + 8192, T2_Z = T2_KV + 2048;
constexpr int K2_LDS = T2_Z + 512;

__global__ __launch_bounds__(512, 2) void k2_kernel(const u16* __restrict__ x, const u16* __restrict__ wqkv, const u16* __restrict__ wo,
                                                    const u16* __restrict__ w2, char* __restrict__ ws) {
  extern __shared__ __attribute__((aligned(16))) char smem[];
  const int b = blockIdx.x, xcd = b & 7, idx = b >> 3;
  const int wave = __builtin_amdgcn_readfirstlane(threadIdx.x >> 6), lane = threadIdx.x & 63;
  const int g = lane >> 4, l15 = lane & 15;
  const bool isq = xcd < 4;
  const int rp = ((xcd >> 1) & 1) * 8 + (idx >> 2), cp = (xcd & 1) * 4 + (idx & 3);
  const int mh = b >> 3, ms = b & 7;
  if (wave >= 4) {
    const int lw = wave - 4;
    u32 vx[4], vw[4];
    x_voffs<4>(lane, lw, DM, vx);
    const int ldw = isq ? 3072 : DM;
#pragma unroll
    for (int j = 0; j < 4; ++j) {
      int k, c; w256_src(lane, lw * 4 + j, k, c);
      vw[j] = (u32)(k * ldw + cp * 128 + 8 * c) * 2u;
    }
#pragma unroll
    for (int j = 0; j < 2; ++j) {
      int k, c; w256_src(lane, lw * 2 + j, k, c);
      glds16(T2_WO + (u32)(lw * 2 + j) * 1024u, (u32)((32 * mh + k) * DM + ms * 128 + 8 * c) * 2u, wo);
    }
    if (isq) {
      const char* xb0 = (const char*)(x + (size_t)rp * 128 * DM);
      const char* wb0 = (const char*)wqkv;
      auto addr = [&](int t, const char*& xb, const char*& wb) { xb = xb0 + t * 128; wb = wb0 + (size_t)t * (64 * 3072 * 2); };
      loader_loop<4, 4, 4, 16384, 32768>(16, lw, vx, vw, true, true, addr);
    } else {
      const char* xb0 = ws + OFF_H + (size_t)(2 + rp * 128) * DM * 2;
      const char* wb0 = (const char*)w2;
      auto addr = [&](int t, const char*& xb, const char*& wb) {
        const int q = t >> 2;
        const int sh = (q & 2 ? 2 : 1) * (q & 1 ? 1 : -1);
        xb = xb0 + (ptrdiff_t)sh * (DM * 2) + t * 128;
        wb = wb0 + (size_t)t * (64 * DM * 2);
      };
      loader_loop<4, 4, 4, 16384, 32768>(16, lw, vx, vw, true, true, addr);
    }
    return;
  }
  const int wm = wave >> 1, wn = wave & 1, tid = threadIdx.x;
  {
    const float* kvp = (const float*)(ws + OFF_KVP) + (size_t)mh * 1024 + tid * 4;
    f32x4 s = *(const f32x4*)kvp;
#pragma unroll
    for (int p = 1; p < 8; ++p) s += *(const f32x4*)(kvp + (size_t)p * 32 * 1024);
    *(LAS u32x2*)(T2_KV + tid * 8) = (u32x2){pk2(s[0], s[1]), pk2(s[2], s[3])};
    if (isq && tid < 128) {
      const float* zp = (const float*)(ws + OFF_ZP) + cp * 128 + tid;
      float zs = zp[0];
#pragma unroll
      for (int p = 1; p < 8; ++p) zs += zp[p * 1024];
      *(LAS float*)(T2_Z + tid * 4) = zs;
    }
  }
  u32 xa0, xa1, wa[4];
  x_addrs(lane, wm * 64, xa0, xa1);
  w_addrs<4, 256>(lane, wn * 4, 16384, wa);
  const float* gate = (const float*)(ws + OFF_G) + rp * 128 + wm * 64 + l15;
  float gt[4];
#pragma unroll
  for (int m = 0; m < 4; ++m) gt[m] = gate[m * 16];
  f32x4 acc[4][4];
#pragma unroll
  for (int m = 0; m < 4; ++m)
#pragma unroll
    for (int n = 0; n < 4; ++n) acc[m][n] = (f32x4){0.f, 0.f, 0.f, 0.f};
  wait_lgkm0();
  bar();
  {
    u32 oa[2];
    w_addrs<2, 256>(lane, wave * 2, T2_WO, oa);
    bf16x8 wf[2];
    read_w<2, 256>(wf, oa, 0);
    u16* mo = (u16*)(ws + OFF_M) + (size_t)(32 * mh + l15) * DM + ms * 128 + wave * 32 + 16 * (g & 1) + 8 * (g >> 1);
#pragma unroll
    for (int i = 0; i < 2; ++i) {
      const bf16x8 kf = lds_b128(T2_KV + (16 * i + l15) * 64 + g * 16);
      const f32x4 zero = {0.f, 0.f, 0.f, 0.f};
      const f32x4 m0 = mfma(wf[0], kf, zero), m1 = mfma(wf[1], kf, zero);
      *(u32x4*)(mo + (size_t)16 * i * DM) = pack_pair(m0, m1);
    }
  }
  NoExtra ex;
  compute_loop<4, 4, 256, 4, 32768, true, 1>(acc, 16, xa0, xa1, wa, ex);
  const int row = rp * 128 + wm * 64 + l15, col = cp * 128 + wn * 64 + 16 * (g & 1) + 8 * (g >> 1);
  if (isq) {
    f32x4 zt[4];
#pragma unroll
    for (int n = 0; n < 4; ++n) zt[n] = *(LAS f32x4*)(T2_Z + (wn * 64 + n * 16 + 4 * g) * 4);
    u16* pg = (u16*)(ws + OFF_PG) + (size_t)row * KPG + col;
#pragma unroll
    for (int m = 0; m < 4; ++m) {
#pragma unroll
      for (int hh = 0; hh < 2; ++hh) {
        f32x4 a = acc[m][2 * hh], c = acc[m][2 * hh + 1];
        float den = 0.f;
#pragma unroll
        for (int r = 0; r < 4; ++r) { a[r] = phi(a[r]); c[r] = phi(c[r]); den += a[r] * zt[2 * hh][r] + c[r] * zt[2 * hh + 1][r]; }
        den = grp_sum(den) + 1e-6f;
        const float sc = gt[m] / den;
        *(u32x4*)(pg + (size_t)m * 16 * KPG + 32 * hh) = pack_pair(a * sc, c * sc);
      }
    }
  } else {
    const u16* xr = x + (size_t)row * DM + col;
    u16* x1 = (u16*)(ws + OFF_X1) + (size_t)row * DM + col;
    f32x2* st = (f32x2*)(ws + OFF_ST) + (size_t)row * 16 + cp * 2 + wn;
    u32x4 xv[4][2];
#pragma unroll
    for (int m = 0; m < 4; ++m) { xv[m][0] = *(const u32x4*)(xr + (size_t)m * 16 * DM); xv[m][1] = *(const u32x4*)(xr + (size_t)m * 16 * DM + 32); }
#pragma unroll
    for (int m = 0; m < 4; ++m) {
      float v[16];
#pragma unroll
      for (int hh = 0; hh < 2; ++hh) {
        f32x4 a = acc[m][2 * hh], c = acc[m][2 * hh + 1];
        swap_pair_f32(a, c);
        const u32x4 xx = xv[m][hh];
        const u32x4 o = {pk2(a[0] + bf_lo(xx[0]), a[1] + bf_hi(xx[0])), pk2(a[2] + bf_lo(xx[1]), a[3] + bf_hi(xx[1])),
                         pk2(c[0] + bf_lo(xx[2]), c[1] + bf_hi(xx[2])), pk2(c[2] + bf_lo(xx[3]), c[3] + bf_hi(xx[3]))};
        *(u32x4*)(x1 + (size_t)m * 16 * DM + 32 * hh) = o;
#pragma unroll
        for (int e = 0; e < 4; ++e) { v[8 * hh + 2 * e] = bf_lo(o[e]); v[8 * hh + 2 * e + 1] = bf_hi(o[e]); }
      }
      float s = 0.f;
#pragma unroll
      for (int e = 0; e < 16; ++e) s += v[e];
      const float mean = grp_sum(s) * (1.0f / 64.0f);
      float q = 0.f;
#pragma unroll
      for (int e = 0; e < 16; ++e) q += (v[e] - mean) * (v[e] - mean);
      q = grp_sum(q);
      if (g == 0) st[(size_t)m * 16 * 16] = (f32x2){mean, q};
    }
  }
}

constexpr u32 T3_CS = 3 * 49152;
constexpr int K3_LDS = T3_CS + 512;

__global__ __launch_bounds__(512, 2) void k3_kernel(const u16* __restrict__ w3, char* __restrict__ ws) {
  extern __shared__ __attribute__((aligned(16))) char smem[];
  const int b = blockIdx.x, xcd = b & 7, idx = b >> 3;
  const int wave = __builtin_amdgcn_readfirstlane(threadIdx.x >> 6), lane = threadIdx.x & 63;
  const int g = lane >> 4, l15 = lane & 15;
  const int rp = (xcd >> 2) * 4 + (idx >> 3), cp = (xcd & 3) * 8 + (idx & 7);
  if (wave >= 4) {
    const int lw = wave - 4;
    u32 vx[8], vw[4];
    x_voffs<8>(lane, lw, DM, vx);
    bool val[2];
#pragma unroll
    for (int j = 0; j < 4; ++j) {
      int k, c; w256_src(lane, lw * 4 + j, k, c);
      vw[j] = (u32)(k * FF + cp * 112 + 8 * c) * 2u;
      val[j >> 1] = c < 14;
    }
    const char* xb0 = ws + OFF_X1 + (size_t)rp * 256 * DM * 2;
    const char* wb0 = (const char*)w3;
    auto addr = [&](int t, const char*& xb, const char*& wb) { xb = xb0 + t * 128; wb = wb0 + (size_t)t * (64 * FF * 2); };
    loader_loop<8, 4, 3, 32768, 49152>(16, lw, vx, vw, val[0], val[1], addr);
    return;
  }
  const int tid = threadIdx.x;
  if (tid < 28) {
    const float* cs = (const float*)(ws + OFF_CS3) + cp * 112 + tid * 4;
    *(LAS f32x4*)(T3_CS + tid * 16) = (*(const f32x4*)cs + *(const f32x4*)(cs + FF)) + (*(const f32x4*)(cs + 2 * FF) + *(const f32x4*)(cs + 3 * FF));
  }
  const int row = rp * 256 + wave * 64 + l15;
  float rs[4], mr[4], og[4];
#pragma unroll
  for (int m = 0; m < 4; ++m) {
    const f32x4* st = (const f32x4*)(ws + OFF_ST + (size_t)(row + m * 16) * 128 + g * 32);
    const f32x4 a = st[0], c = st[1];
    const float mean = grp_sum((a[0] + a[2]) + (c[0] + c[2])) * (1.0f / 16.0f);
    const float d0 = a[0] - mean, d1 = a[2] - mean, d2 = c[0] - mean, d3 = c[2] - mean;
    const float m2 = grp_sum((a[1] + a[3]) + (c[1] + c[3]) + 64.0f * ((d0 * d0 + d1 * d1) + (d2 * d2 + d3 * d3)));
    rs[m] = __builtin_amdgcn_rsqf(m2 * (1.0f / DM) + 1e-5f);
    mr[m] = mean * rs[m];
    og[m] = 1.0f - ((const float*)(ws + OFF_G))[row + m * 16];
  }
  u32 xa0, xa1, wa[7];
  x_addrs(lane, wave * 64, xa0, xa1);
  w_addrs<7, 256>(lane, 0, 32768, wa);
  f32x4 acc[4][7];
#pragma unroll
  for (int m = 0; m < 4; ++m)
#pragma unroll
    for (int n = 0; n < 7; ++n) acc[m][n] = (f32x4){0.f, 0.f, 0.f, 0.f};
  NoExtra ex;
  wait_lgkm0();
  bar();
  compute_loop<4, 7, 256, 3, 49152, true, 1>(acc, 16, xa0, xa1, wa, ex);
  f32x4 cs[7];
#pragma unroll
  for (int n = 0; n < 7; ++n) cs[n] = *(LAS f32x4*)(T3_CS + (n * 16 + 4 * g) * 4);
  u16* pg = (u16*)(ws + OFF_PG) + (size_t)row * KPG + DM + cp * 112;
  const int co = 16 * (g & 1) + 8 * (g >> 1);
#pragma unroll
  for (int m = 0; m < 4; ++m) {
#pragma unroll
    for (int n = 0; n < 7; ++n)
#pragma unroll
      for (int r = 0; r < 4; ++r) acc[m][n][r] = og[m] * gelu_tanh(acc[m][n][r] * rs[m] - mr[m] * cs[n][r]);
    u16* o = pg + (size_t)m * 16 * KPG;
#pragma unroll
    for (int p = 0; p < 3; ++p) *(u32x4*)(o + 32 * p + co) = pack_pair(acc[m][2 * p], acc[m][2 * p + 1]);
    *(u32x2*)(o + 96 + 4 * g) = (u32x2){pk2(acc[m][6][0], acc[m][6][1]), pk2(acc[m][6][2], acc[m][6][3])};
  }
}

constexpr int K4_LDS = 6 * 24576;

__global__ __launch_bounds__(512, 2) void k4_kernel(const u16* __restrict__ w4, const char* __restrict__ ws, u16* __restrict__ out) {
  extern __shared__ __attribute__((aligned(16))) char smem[];
  const int b = blockIdx.x, xcd = b & 7, idx = b >> 3;
  const int wave = __builtin_amdgcn_readfirstlane(threadIdx.x >> 6), lane = threadIdx.x & 63;
  const int g = lane >> 4, l15 = lane & 15;
  const int rp = (xcd >> 1) * 4 + (idx >> 3), cp = (xcd & 1) * 8 + (idx & 7);
  if (wave >= 4) {
    const int lw = wave - 4;
    u32 vx[4], vw[2];
    x_voffs<4>(lane, lw, KPG, vx);
#pragma unroll
    for (int j = 0; j < 2; ++j) {
      int k, c; w128_src(lane, lw * 2 + j, k, c);
      vw[j] = (u32)(k * DM + cp * 64 + 8 * c) * 2u;
    }
    const char* xb0 = ws + OFF_PG + (size_t)rp * 128 * KPG * 2;
    const char* wm0 = ws + OFF_M;
    const char* w40 = (const char*)w4 - (size_t)DM * DM * 2;
    auto addr = [&](int t, const char*& xb, const char*& wb) { xb = xb0 + t * 128; wb = (t < 16 ? wm0 : w40) + (size_t)t * (64 * DM * 2); };
    loader_loop<4, 2, 6, 16384, 24576>(KPG / 64, lw, vx, vw, true, true, addr);
    return;
  }
  const int wm = wave >> 1, wn = wave & 1;
  const int row = rp * 128 + wm * 64 + l15, col = cp * 64 + wn * 32 + 16 * (g & 1) + 8 * (g >> 1);
  u32x4 xv[4];
  float og[4];
#pragma unroll
  for (int m = 0; m < 4; ++m) {
    xv[m] = *(const u32x4*)((const u16*)(ws + OFF_X1) + (size_t)(row + m * 16) * DM + col);
    og[m] = 1.0f - ((const float*)(ws + OFF_G))[row + m * 16];
  }
  u32 xa0, xa1, wa[2];
  x_addrs(lane, wm * 64, xa0, xa1);
  w_addrs<2, 128>(lane, wn * 2, 16384, wa);
  f32x4 acc[4][2];
#pragma unroll
  for (int m = 0; m < 4; ++m) { acc[m][0] = (f32x4){0.f, 0.f, 0.f, 0.f}; acc[m][1] = acc[m][0]; }
  NoExtra ex;
  wait_lgkm0();
  bar();
  compute_loop<4, 2, 128, 6, 24576, true, 1>(acc, KPG / 64, xa0, xa1, wa, ex);
#pragma unroll
  for (int m = 0; m < 4; ++m) {
    f32x4 a = acc[m][0], c = acc[m][1];
    swap_pair_f32(a, c);
    const u32x4 xx = xv[m];
    const float o = og[m];
    *(u32x4*)(out + (size_t)(row + m * 16) * DM + col) =
        (u32x4){pk2(a[0] + o * bf_lo(xx[0]), a[1] + o * bf_hi(xx[0])), pk2(a[2] + o * bf_lo(xx[1]), a[3] + o * bf_hi(xx[1])),
                pk2(c[0] + o * bf_lo(xx[2]), c[1] + o * bf_hi(xx[2])), pk2(c[2] + o * bf_lo(xx[3]), c[3] + o * bf_hi(xx[3]))};
  }
}

}

extern "C" void kernel_launch(void* const* d_in, const int* in_sizes, int n_in, void* d_out, int out_size, void* d_ws, size_t ws_size,
                              hipStream_t stream) {
  const u16* x = (const u16*)d_in[0];
  const u16* wqkv = (const u16*)d_in[1];
  const u16* wo = (const u16*)d_in[2];
  const u16* w1 = (const u16*)d_in[4];
  const u16* w2 = (const u16*)d_in[5];
  const u16* w3 = (const u16*)d_in[6];
  const u16* w4 = (const u16*)d_in[7];
  const u16* gw = (const u16*)d_in[8];
  char* ws = (char*)d_ws;
  (void)hipFuncSetAttribute((const void*)k1_kernel, hipFuncAttributeMaxDynamicSharedMemorySize, K1_LDS);
  (void)hipFuncSetAttribute((const void*)k2_kernel, hipFuncAttributeMaxDynamicSharedMemorySize, K2_LDS);
  (void)hipFuncSetAttribute((const void*)k3_kernel, hipFuncAttributeMaxDynamicSharedMemorySize, K3_LDS);
  (void)hipFuncSetAttribute((const void*)k4_kernel, hipFuncAttributeMaxDynamicSharedMemorySize, K4_LDS);
  k1_kernel<<<256, 512, K1_LDS, stream>>>(x, wqkv, w1, w3, gw, ws);
  k2_kernel<<<256, 512, K2_LDS, stream>>>(x, wqkv, wo, w2, ws);
  k3_kernel<<<256, 512, K3_LDS, stream>>>(w3, ws);
  k4_kernel<<<256, 512, K4_LDS, stream>>>(w4, ws, (u16*)d_out);
}
```

```cpp
#include <hip/hip_runtime.h>
#include <hip/hip_bf16.h>
#include <stdint.h>

namespace {

typedef unsigned short u16;
typedef unsigned int u32;
typedef short s16x4 __attribute__((ext_vector_type(4)));
typedef short s16x8 __attribute__((ext_vector_type(8)));
typedef __bf16 bf16x8 __attribute__((ext_vector_type(8)));
typedef __bf16 bf16x2 __attribute__((ext_vector_type(2)));
typedef float f32x2 __attribute__((ext_vector_type(2)));
typedef float f32x4 __attribute__((ext_vector_type(4)));
typedef u32 u32x2 __attribute__((ext_vector_type(2)));
typedef u32 u32x4 __attribute__((ext_vector_type(4)));
#define LAS __attribute__((address_space(3)))
#define GAS __attribute__((address_space(1)))
#define DI __device__ __forceinline__

constexpr int SEQ = 2048, DM = 1024, FF = 3584, KPG = DM + FF;

constexpr size_t OFF_H   = 0;
constexpr size_t OFF_X1  = OFF_H + (size_t)(SEQ + 4) * DM * 2;
constexpr size_t OFF_PG  = OFF_X1 + (size_t)SEQ * DM * 2;
constexpr size_t OFF_M   = OFF_PG + (size_t)SEQ * KPG * 2;
constexpr size_t OFF_KVP = OFF_M + (size_t)DM * DM * 2;
constexpr size_t OFF_ZP  = OFF_KVP + (size_t)8 * 32 * 1024 * 4;
constexpr size_t OFF_G   = OFF_ZP + (size_t)8 * 32 * 32 * 4;
constexpr size_t OFF_ST  = OFF_G + (size_t)SEQ * 4;
constexpr size_t OFF_CS3 = OFF_ST + (size_t)SEQ * 16 * 8;
constexpr size_t OFF_FLG = OFF_CS3 + (size_t)4 * FF * 4;
constexpr size_t OFF_PRT = OFF_FLG + (size_t)128 * 128;

#define STAMP(kid, i) do {} while (0)

#define TICK() do {} while (0)
#define TICK_INIT(kid) do {} while (0)
#define GA84 24
#define BI84 4
#define GA44 12
#define BI44 4
#define GA47 18
#define BI47 4
#define K4_NH 33

DI u32 pk2(float a, float b) { bf16x2 v = __builtin_convertvector((f32x2){a, b}, bf16x2); return __builtin_bit_cast(u32, v); }
DI float bf_lo(u32 u) { return __uint_as_float(u << 16); }
DI float bf_hi(u32 u) { return __uint_as_float(u & 0xffff0000u); }
DI bf16x8 lds_b128(u32 a) { return *(const LAS bf16x8*)a; }
DI s16x4 lds_tr(u32 a) { return __builtin_amdgcn_ds_read_tr16_b64_v4i16((LAS s16x4*)a); }
DI bf16x8 cat8(s16x4 a, s16x4 b) { return __builtin_bit_cast(bf16x8, __builtin_shufflevector(a, b, 0, 1, 2, 3, 4, 5, 6, 7)); }
DI f32x4 mfma(bf16x8 a, bf16x8 b, f32x4 c) { return __builtin_amdgcn_mfma_f32_16x16x32_bf16(a, b, c, 0, 0, 0); }
DI void bar() { asm volatile("" ::: "memory"); __builtin_amdgcn_s_barrier(); asm volatile("" ::: "memory"); }
DI void wait_lgkm0() { asm volatile("" ::: "memory"); __builtin_amdgcn_s_waitcnt(0xC07F); asm volatile("" ::: "memory"); }
template <int N> DI void wait_vm() { asm volatile("s_waitcnt vmcnt(%0)" ::"n"(N) : "memory"); }
#define SCHED() __builtin_amdgcn_sched_barrier(0)

DI void glds16(u32 lds, u32 voff, const void* gbase) {
  u32 keep;
  asm volatile("s_mov_b32 %0, m0\n\ts_mov_b32 m0, %1\n\ts_nop 3\n\tglobal_load_lds_dwordx4 %2, %3\n\ts_mov_b32 m0, %0"
               : "=&s"(keep) : "s"(__builtin_amdgcn_readfirstlane(lds)), "v"(voff), "s"(gbase) : "memory");
}
DI void glds4(u32 lds, u32 voff, const void* gbase) {
  u32 keep;
  asm volatile("s_mov_b32 %0, m0\n\ts_mov_b32 m0, %1\n\ts_nop 3\n\tglobal_load_lds_dword %2, %3\n\ts_mov_b32 m0, %0"
               : "=&s"(keep) : "s"(__builtin_amdgcn_readfirstlane(lds)), "v"(voff), "s"(gbase) : "memory");
}
DI void touch(u32 lds, u32 voff, const void* gbase) { glds4(lds, voff, gbase); }
template <class T> DI void ld16(T& v, const void* base, u32 voff) { asm volatile("global_load_dwordx4 %0, %1, %2" : "=v"(v) : "v"(voff), "s"(base) : "memory"); }
template <class T> DI void ld16c(T& v, const void* base, u32 voff) { asm volatile("global_load_dwordx4 %0, %1, %2 sc1" : "=v"(v) : "v"(voff), "s"(base) : "memory"); }
DI void ld4(float& v, const void* base, u32 voff) { asm volatile("global_load_dword %0, %1, %2" : "=v"(v) : "v"(voff), "s"(base) : "memory"); }
DI void st16c(void* base, u32 voff, u32x4 v) { asm volatile("global_store_dwordx4 %0, %1, %2 sc1\n\ts_nop 1" ::"v"(voff), "v"(v), "s"(base) : "memory"); }
DI void st16(void* base, u32 voff, u32x4 v) { asm volatile("global_store_dwordx4 %0, %1, %2\n\ts_nop 1" ::"v"(voff), "v"(v), "s"(base) : "memory"); }
#define WT 0
DI void st16w(void* p, u32x4 v) {
  *(u32x4*)p = v;
}
DI void st8w(void* p, u32x2 v) {
  *(u32x2*)p = v;
}
template <class T> DI void pin(T& v) { asm volatile("; pin %0" : "+v"(v)); }

DI float grp_sum(float v) {
  u32 u = __float_as_uint(v);
  auto r = __builtin_amdgcn_permlane16_swap(u, u, false, false);
  float s = __uint_as_float(r[0]) + __uint_as_float(r[1]);
  u = __float_as_uint(s);
  auto q = __builtin_amdgcn_permlane32_swap(u, u, false, false);
  return __uint_as_float(q[0]) + __uint_as_float(q[1]);
}
template <int N> DI float ror_add(float v) {
  return v + __uint_as_float(__builtin_amdgcn_update_dpp(0u, __float_as_uint(v), 0x120 + N, 0xf, 0xf, false));
}

DI float gelu_tanh(float x) {
  const float c0 = -2.0f * 0.7978845608028654f * 1.4426950408889634f, c1 = c0 * 0.044715f;
  const float e = __builtin_amdgcn_exp2f(x * (c0 + c1 * x * x));
  return x * __builtin_amdgcn_rcpf(1.0f + e);
}
DI float phi(float t) { return (t > 0.0f ? t + 1.0f : __builtin_amdgcn_exp2f(t * 1.4426950408889634f)) + 1e-6f; }

DI void swap_pair_f32(f32x4& a, f32x4& b) {
#pragma unroll
  for (int r = 0; r < 4; ++r) {
    auto s = __builtin_amdgcn_permlane16_swap(__float_as_uint(a[r]), __float_as_uint(b[r]), false, false);
    a[r] = __uint_as_float(s[0]); b[r] = __uint_as_float(s[1]);
  }
}
DI u32x4 pack_pair(const f32x4& a, const f32x4& b) {
  auto s0 = __builtin_amdgcn_permlane16_swap(pk2(a[0], a[1]), pk2(b[0], b[1]), false, false);
  auto s1 = __builtin_amdgcn_permlane16_swap(pk2(a[2], a[3]), pk2(b[2], b[3]), false, false);
  return (u32x4){s0[0], s1[0], s0[1], s1[1]};
}
DI u32x4 axpy_pack(f32x4 a, f32x4 c, float s, u32x4 x) {
  return (u32x4){pk2(a[0] + s * bf_lo(x[0]), a[1] + s * bf_hi(x[0])), pk2(a[2] + s * bf_lo(x[1]), a[3] + s * bf_hi(x[1])),
                 pk2(c[0] + s * bf_lo(x[2]), c[1] + s * bf_hi(x[2])), pk2(c[2] + s * bf_lo(x[3]), c[3] + s * bf_hi(x[3]))};
}

template <int I> struct IC { static constexpr int v = I; };
template <int P, int R, int S, int NCS, int LAST, class Side> DI void loader_tail(const Side& side) {
  constexpr int c = S - 2 - R;
  side(IC<c>{});
  if constexpr (R >= LAST) {
    wait_vm<P * R + NCS % 100 + (c >= 1 ? NCS / 100 % 100 : 0) + (c >= 2 ? NCS / 10000 : 0)>();
    bar();
    if constexpr (R > 0) loader_tail<P, R - 1, S, NCS, LAST>(side);
  }
}
template <int NPX, int NPW, int S, int XBYTES, int STAGE, int NE, int NCS, int LAST, class Addr, class Early, class Side>
DI void loader_loop(int NT, int lw, const u32 (&vx)[NPX], const u32 (&vw)[NPW], bool wvalid0, bool wvalid1, const Addr& addr,
                    const Early& early, const Side& side) {
  constexpr int P = NPX + NPW;
  auto issue = [&](int t, int st) {
    const char *xb, *wb;
    addr(t, xb, wb);
    const u32 base = (u32)st * STAGE;
#pragma unroll
    for (int j = 0; j < NPX; ++j) glds16(base + (u32)(lw * NPX + j) * 1024u, vx[j], xb);
#pragma unroll
    for (int j = 0; j < NPW; ++j) {
      if ((j & 2) ? wvalid1 : wvalid0) glds16(base + XBYTES + (u32)(lw * NPW + j) * 1024u, vw[j], wb);
    }
  };
  issue(0, 0);
  issue(1, 1);
  wait_vm<P>();
  bar();
#pragma unroll
  for (int t = 2; t < S; ++t) issue(t, t);
  early();
  int st = 0;
#pragma unroll 1
  for (int t = 1; t <= NT - S; ++t) {
    if (NE > 0 && t < S) wait_vm<P*(S - 2) + NE>(); else wait_vm<P*(S - 2)>();
    bar();
    issue(t - 1 + S, st);
    st = (st + 1 == S) ? 0 : st + 1;
  }
  loader_tail<P, S - 2, S, NCS, LAST>(side);
}

struct NoExtra {
  static constexpr int N0 = 0, N1 = 0;
  template <int KS, int I, class F> DI void step(const F&) {}
};
template <int WM_T, int WN_T> struct Frags {
  bf16x8 x[WM_T];
  s16x4 lo[WN_T], hi[WN_T];
  DI bf16x8 w(int n) const { return cat8(lo[n], hi[n]); }
};
template <int WN_T> struct Addrs { u32 a[WN_T + 2]; };

template <int R, int KS, int WM_T, int WN_T>
DI void rd_one(Frags<WM_T, WN_T>& f, const Addrs<WN_T>& ad) {
  if constexpr (R == 0) f.x[0] = lds_b128(ad.a[KS]);
  else if constexpr (R <= 2 * WN_T) {
    constexpr int n = (R - 1) >> 1;
    if constexpr ((R - 1) & 1) f.hi[n] = lds_tr(ad.a[2 + n] + (KS * 32 + 4) * 256); else f.lo[n] = lds_tr(ad.a[2 + n] + KS * 32 * 256);
  } else if constexpr (R < WM_T + 2 * WN_T) f.x[R - 2 * WN_T] = lds_b128(ad.a[KS] + (R - 2 * WN_T) * 2048);
}
template <int R, int RE, int KS, int WM_T, int WN_T>
DI void rd_range(Frags<WM_T, WN_T>& f, const Addrs<WN_T>& ad) {
  if constexpr (R < RE) { rd_one<R, KS, WM_T, WN_T>(f, ad); rd_range<R + 1, RE, KS, WM_T, WN_T>(f, ad); }
}
template <int R, int RE, int WN_T> DI void adv_range(Addrs<WN_T>& ad, u32 d) {
  if constexpr (R < RE) { ad.a[R] += d; pin(ad.a[R]); adv_range<R + 1, RE>(ad, d); }
}

template <int I, int KS, int WM_T, int WN_T, bool ORIENT_T, int G0, int G1, bool MORE, class Extra>
DI void sub_step(f32x4 (&acc)[WM_T][WN_T], const Frags<WM_T, WN_T>& c, Frags<WM_T, WN_T>& n, Addrs<WN_T>& ad, u32 d, Extra& ex) {
  constexpr int NM = WM_T * WN_T, NS = NM + (KS ? Extra::N1 : Extra::N0), NR = WM_T + 2 * WN_T;
  if constexpr (I < NS) {
    if constexpr (KS == 1 && MORE && I == G0) { wait_lgkm0(); bar(); TICK(); }
    if constexpr (I < NM) {
      constexpr int m = I / WN_T, j = I % WN_T;
      acc[m][j] = ORIENT_T ? mfma(c.w(j), c.x[m], acc[m][j]) : mfma(c.x[m], c.w(j), acc[m][j]);
    } else {
      ex.template step<KS, I - NM>(c);
    }
    if constexpr ((KS == 0 || MORE) && I >= G0 && I < G1) {
      SCHED();
      rd_range<(I - G0) * NR / (G1 - G0), (I + 1 - G0) * NR / (G1 - G0), 1 - KS, WM_T, WN_T>(n, ad);
    }
    if constexpr (KS == 0 && MORE && I >= G1) adv_range<(I - G1) * (WN_T + 2) / (NS - G1), (I + 1 - G1) * (WN_T + 2) / (NS - G1)>(ad, d);
    SCHED();
    sub_step<I + 1, KS, WM_T, WN_T, ORIENT_T, G0, G1, MORE>(acc, c, n, ad, d, ex);
  }
}

template <int WM_T, int WN_T, int S, int STAGE, bool ORIENT_T, int G1A, int BARI, class Extra>
DI void compute_loop(f32x4 (&acc)[WM_T][WN_T], int NT, int lane, int xrow0, u32 wbase, int nt0, Extra& ex) {
  constexpr int NM = WM_T * WN_T;
  Frags<WM_T, WN_T> F0, F1;
  Addrs<WN_T> ad;
  {
    const u32 g = lane >> 4, f = lane & 7, r = (u32)(xrow0 + (lane & 15)) * 128u;
    ad.a[0] = r + ((g ^ f) << 4);
    ad.a[1] = r + (((4 + g) ^ f) << 4);
    const u32 q = (lane & 15) >> 2, p = lane & 3, xm = (q | ((g & 1) << 2)) << 1;
#pragma unroll
    for (int n = 0; n < WN_T; ++n) ad.a[2 + n] = wbase + (8 * g + q) * 256 + (((u32)(2 * (nt0 + n)) + (p >> 1)) ^ xm) * 16u + (p & 1) * 8u;
  }
  adv_range<0, WN_T + 2>(ad, 0u);
#pragma unroll
  for (int m = 0; m < WM_T; ++m)
#pragma unroll
    for (int n = 0; n < WN_T; ++n) { acc[m][n] = (f32x4){0.f, 0.f, 0.f, 0.f}; pin(acc[m][n]); }
  wait_lgkm0();
  bar();
  ex.start();
  rd_range<0, WM_T + 2 * WN_T, 0, WM_T, WN_T>(F0, ad);
  int st = 1;
  for (int t = 0; t < NT - 1; ++t) {
    const u32 d = (st == S) ? (u32)(-(S - 1) * STAGE) : (u32)STAGE;
    st = (st == S) ? 1 : st + 1;
    sub_step<0, 0, WM_T, WN_T, ORIENT_T, 0, G1A, true>(acc, F0, F1, ad, d, ex);
    sub_step<0, 1, WM_T, WN_T, ORIENT_T, BARI, NM + Extra::N1, true>(acc, F1, F0, ad, d, ex);
  }
  sub_step<0, 0, WM_T, WN_T, ORIENT_T, 0, G1A, false>(acc, F0, F1, ad, 0u, ex);
  sub_step<0, 1, WM_T, WN_T, ORIENT_T, 0, 1, false>(acc, F1, F0, ad, 0u, ex);
}
template <class F> struct StartExtra : NoExtra {
  const F& f;
  DI void start() { f(); }
};
#define EXTRA(kid_, f_) StartExtra<decltype(f_)> ex{{}, f_}

template <int NPX> DI void x_voffs(int lane, int lw, int ld, u32 (&vx)[NPX]) {
#pragma unroll
  for (int j = 0; j < NPX; ++j) vx[j] = (u32)((8 * (lw * NPX + j) + (lane >> 3)) * ld) * 2u + (u32)(((lane & 7) ^ (lane >> 3)) << 4);
}
DI void w256_src(int lane, int piece, int& k, int& c) {
  k = 4 * piece + (lane >> 4);
  c = (lane & 15) ^ (((k & 3) | (((k >> 3) & 1) << 2)) << 1);
}
template <int WN_T> DI void give_rows(const f32x4 (&acc)[4][WN_T], f32x4 (&e)[2][WN_T], u32 slot) {
#pragma unroll
  for (int m = 0; m < 2; ++m)
#pragma unroll
    for (int n = 0; n < WN_T; ++n) { *(LAS f32x4*)(slot + (m * WN_T + n) * 1024) = acc[2 + m][n]; e[m][n] = acc[m][n]; }
}
template <int WN_T> DI void take_rows(f32x4 (&e)[2][WN_T], u32 slot) {
#pragma unroll
  for (int m = 0; m < 2; ++m)
#pragma unroll
    for (int n = 0; n < WN_T; ++n) e[m][n] = *(LAS f32x4*)(slot + (m * WN_T + n) * 1024);
}

DI bf16x8 ones8() { const u32 o = 0x3f803f80u; return __builtin_bit_cast(bf16x8, (u32x4){o, o, o, o}); }

constexpr u32 K1_DUMMY = 3 * 49152;
constexpr int K1_LDS = K1_DUMMY + 256;
constexpr u32 T1_GW = 131072, T1_ONE = T1_GW + 2048, T1_SUM = T1_ONE + 64, T1_SSQ = T1_SUM + 512, T1_CS = T1_SSQ + 512;

template <int WM, int WN> struct StatExtra {
  static constexpr int N0 = WM == 0 ? 8 : 4, N1 = WM == 1 ? 8 : 4;
  f32x4 (&racc)[4]; f32x4 (&cacc)[4];
  u32 sa, sstep;
  bf16x8 a;
  DI void start() { STAMP(0, 1); auto& ex = *this; TICK_INIT(0); if (WN == 1) a = lds_b128(sa); }
  template <int KS, int I, class F> DI void step(const F& f) {
    if constexpr (I < 4) {
      if constexpr (WN == 0) racc[I] = mfma(f.x[I], f.x[I], racc[I]);
      else {
        racc[I] = mfma(a, f.x[I], racc[I]);
        if constexpr (I == 3) { sa += sstep; a = lds_b128(sa); }
      }
    } else {
      cacc[I - 4] = mfma(f.w(I - 4), ones8(), cacc[I - 4]);
    }
  }
};
template <int WM, int WN>
DI void w1_loop(f32x4 (&acc)[4][4], f32x4 (&racc)[4], f32x4 (&cacc)[4], u32 sa, u32 sstep, int lane, char* ws) {
  StatExtra<WM, WN> ex{racc, cacc, sa, sstep, ones8()};
  compute_loop<4, 4, 4, 32768, true, GA44, BI44>(acc, 16, lane, WM * 64, 16384, WN * 4, ex);
}

__global__ __launch_bounds__(512, 2) void k1_kernel(const u16* __restrict__ x, const u16* __restrict__ wqkv, const u16* __restrict__ w1,
                                                    const u16* __restrict__ w3, const u16* __restrict__ gw, const u16* __restrict__ wo,
                                                    const u16* __restrict__ w2, char* __restrict__ ws) {
  extern __shared__ __attribute__((aligned(16))) char smem[];
  const int b = blockIdx.x, xcd = b & 7, idx = b >> 3;
  const int wave = __builtin_amdgcn_readfirstlane(threadIdx.x >> 6), lane = threadIdx.x & 63;
  const int g = lane >> 4, l15 = lane & 15;
  STAMP(0, 0);
  auto none = []() {};
  if (idx < 16) {
    const int rp = (xcd >> 1) * 2 + (idx >> 3), hp = (xcd & 1) * 8 + (idx & 7);
    if (wave >= 4) {
      const int lw = wave - 4;
      u32 vx[8], vw[4];
      x_voffs<8>(lane, lw, DM, vx);
#pragma unroll
      for (int j = 0; j < 4; ++j) {
        int k, c; w256_src(lane, lw * 4 + j, k, c);
        vw[j] = (u32)(k * 3072 + 1024 * (1 + ((c >> 2) & 1)) + 32 * (2 * hp + (c >> 3)) + 8 * (c & 3)) * 2u;
      }
      const char* xb0 = (const char*)(x + (size_t)rp * 256 * DM);
      const char* wb0 = (const char*)wqkv;
      auto addr = [&](int t, const char*& xb, const char*& wb) { xb = xb0 + t * 128; wb = wb0 + (size_t)t * (64 * 3072 * 2); };
      auto side = [&](auto c) {
        const int u = (xcd * 16 + idx) * 4 + lw;
        if (c.v == 0) {
          if (u < 256) touch(K1_DUMMY, (u32)((u * 4 + g) * 6144 + l15 * 128), wqkv);
          else touch(K1_DUMMY, (u32)((u - 256) * 8192 + lane * 128), w2);
        } else {
          touch(K1_DUMMY, (u32)(u * 4096 + (lane & 31) * 128), wo);
        }
      };
      loader_loop<8, 4, 3, 32768, 49152, 0, 101, 0>(16, lw, vx, vw, true, true, addr, none, side);
      wait_vm<0>();
      return;
    }
    const int wm = wave >> 1, wn = wave & 1;
    f32x4 acc[8][4];
    EXTRA(0, none);
    compute_loop<8, 4, 3, 49152, false, GA84, BI84>(acc, 16, lane, wm * 128, 32768, wn * 4, ex);
    STAMP(0, 2);
    f32x4 kv[2][2], z[2];
#pragma unroll
    for (int i = 0; i < 2; ++i) { z[i] = (f32x4){0.f, 0.f, 0.f, 0.f}; kv[0][i] = z[i]; kv[1][i] = z[i]; }
    const bf16x8 one = ones8();
#pragma unroll
    for (int mp = 0; mp < 4; ++mp) {
      bf16x8 pkf[2], vf[2];
#pragma unroll
      for (int i = 0; i < 2; ++i) {
        const f32x4 a = acc[2 * mp][i], c = acc[2 * mp + 1][i], d = acc[2 * mp][2 + i], e = acc[2 * mp + 1][2 + i];
        pkf[i] = __builtin_bit_cast(bf16x8, (u32x4){pk2(phi(a[0]), phi(a[1])), pk2(phi(a[2]), phi(a[3])), pk2(phi(c[0]), phi(c[1])), pk2(phi(c[2]), phi(c[3]))});
        vf[i] = __builtin_bit_cast(bf16x8, (u32x4){pk2(d[0], d[1]), pk2(d[2], d[3]), pk2(e[0], e[1]), pk2(e[2], e[3])});
      }
#pragma unroll
      for (int i = 0; i < 2; ++i) {
        kv[0][i] = mfma(vf[0], pkf[i], kv[0][i]);
        kv[1][i] = mfma(vf[1], pkf[i], kv[1][i]);
        z[i] = mfma(one, pkf[i], z[i]);
      }
    }
    LAS f32x4* red = (LAS f32x4*)(u32)(49152 + wn * 5 * 1024 + lane * 16);
    if (wm == 1) {
      red[0] = kv[0][0]; red[64] = kv[0][1]; red[128] = kv[1][0]; red[192] = kv[1][1];
      red[256] = (f32x4){z[0][0], z[1][0], 0.f, 0.f};
    }
    wait_lgkm0();
    bar();
    if (wm == 0) {
      const int head = 2 * hp + wn;
      float* kvp = (float*)(ws + OFF_KVP) + (size_t)(rp * 32 + head) * 1024;
#pragma unroll
      for (int j = 0; j < 2; ++j)
#pragma unroll
        for (int i = 0; i < 2; ++i) *(f32x4*)(kvp + (16 * i + l15) * 32 + 16 * j + 4 * g) = kv[j][i] + red[(2 * j + i) * 64];
      const f32x4 zo = red[256];
      if (g == 0) {
        float* zp = (float*)(ws + OFF_ZP) + (rp * 32 + head) * 32;
        zp[l15] = z[0][0] + zo[0];
        zp[16 + l15] = z[1][0] + zo[1];
      }
    }
    STAMP(0, 3);
    return;
  }
  const int i2 = idx - 16;
  const int rp = (xcd >> 1) * 4 + (i2 >> 2), cp = (xcd & 1) * 4 + (i2 & 3);
  const int w4i = wave & 3, wm = w4i >> 1, wn = w4i & 1;
  const int wb = xcd * 16 + i2;
  LAS float* tsum = (LAS float*)T1_SUM; LAS float* tssq = (LAS float*)T1_SSQ; LAS float* tcs = (LAS float*)T1_CS;
  const u32 slot = (u32)(w4i * 8192 + lane * 16);
  f32x4 e[2][4];
  u32x4 v3[16];
  if (wave >= 4) {
    const int lw = w4i;
    u32 vx[4], vw[4];
    x_voffs<4>(lane, lw, DM, vx);
#pragma unroll
    for (int j = 0; j < 4; ++j) {
      int k, c; w256_src(lane, lw * 4 + j, k, c);
      vw[j] = (u32)(k * DM + cp * 128 + 8 * c) * 2u;
    }
    if (lw < 2) glds16(T1_GW + lw * 1024, (u32)(lw * 1024 + lane * 16), gw);
    const char* xb0 = (const char*)(x + (size_t)rp * 128 * DM);
    const char* wb0 = (const char*)w1;
    auto addr = [&](int t, const char*& xb, const char*& wb_) { xb = xb0 + t * 128; wb_ = wb0 + (size_t)t * (64 * DM * 2); };
    const u32 o3 = (u32)((256 * lw + (lane >> 2)) * FF + min(wb, FF / 32 - 1) * 32 + (lane & 3) * 8) * 2u;
    auto side = [&](auto c) {
#pragma unroll
      for (int i = 4 * c.v; i < 4 * c.v + 4; ++i) ld16(v3[i], w3, o3 + (u32)(16 * i * FF * 2));
    };
    loader_loop<4, 4, 4, 16384, 32768, 0, 40404, 0>(16, lw, vx, vw, true, true, addr, none, side);
    side(IC<3>{});
    bar();
    take_rows<4>(e, slot);
  } else {
    if (threadIdx.x == 0) *(LAS u32x4*)T1_ONE = (u32x4){0x3f803f80u, 0x3f803f80u, 0x3f803f80u, 0x3f803f80u};
    f32x4 acc[4][4], racc[4], cacc[4];
#pragma unroll
    for (int m = 0; m < 4; ++m) { racc[m] = (f32x4){0.f, 0.f, 0.f, 0.f}; cacc[m] = racc[m]; }
    const u32 sa = (l15 == 1) ? T1_GW + g * 16 : T1_ONE, sstep = (l15 == 1) ? 64u : 0u;
    if (wave == 0) w1_loop<0, 0>(acc, racc, cacc, sa, sstep, lane, ws);
    else if (wave == 1) w1_loop<0, 1>(acc, racc, cacc, sa, sstep, lane, ws);
    else if (wave == 2) w1_loop<1, 0>(acc, racc, cacc, sa, sstep, lane, ws);
    else w1_loop<1, 1>(acc, racc, cacc, sa, sstep, lane, ws);
    STAMP(0, 2);
    if (wn == 0) {
      if ((l15 >> 2) == g) {
#pragma unroll
        for (int m = 0; m < 4; ++m) {
          const f32x4 v = racc[m];
          const int r = lane & 3;
          tssq[wm * 64 + m * 16 + l15] = r == 0 ? v[0] : r == 1 ? v[1] : r == 2 ? v[2] : v[3];
        }
      }
    } else if (g == 0) {
      float* gate = (float*)(ws + OFF_G);
#pragma unroll
      for (int m = 0; m < 4; ++m) {
        tsum[wm * 64 + m * 16 + l15] = racc[m][0];
        if (cp == 0) gate[rp * 128 + wm * 64 + m * 16 + l15] = __builtin_amdgcn_rcpf(1.0f + __builtin_amdgcn_exp2f(-1.4426950408889634f * racc[m][1]));
      }
    }
    if (l15 == 0) {
#pragma unroll
      for (int n = 0; n < 4; ++n) *(LAS f32x4*)(tcs + wm * 128 + wn * 64 + n * 16 + 4 * g) = cacc[n];
    }
    give_rows<4>(acc, e, slot);
    wait_lgkm0();
    bar();
  }
  {
    const int r0 = wm * 64 + (wave >> 2) * 32 + l15;
    f32x4 cs[4];
#pragma unroll
    for (int n = 0; n < 4; ++n) cs[n] = *(LAS f32x4*)(tcs + wn * 64 + n * 16 + 4 * g) + *(LAS f32x4*)(tcs + 128 + wn * 64 + n * 16 + 4 * g);
    u16* h = (u16*)(ws + OFF_H) + (size_t)(2 + rp * 128 + r0) * DM + cp * 128 + wn * 64 + 16 * (g & 1) + 8 * (g >> 1);
#pragma unroll
    for (int m = 0; m < 2; ++m) {
      const float mean = tsum[r0 + m * 16] * (1.0f / DM);
      const float var = fmaxf(tssq[r0 + m * 16] * (1.0f / DM) - mean * mean, 0.0f);
      const float rstd = __builtin_amdgcn_rsqf(var + 1e-5f), mr = mean * rstd;
#pragma unroll
      for (int n = 0; n < 4; ++n)
#pragma unroll
        for (int r = 0; r < 4; ++r) e[m][n][r] = gelu_tanh(e[m][n][r] * rstd - mr * cs[n][r]);
      st16w(h + (size_t)m * 16 * DM, pack_pair(e[m][0], e[m][1]));
      st16w(h + (size_t)m * 16 * DM + 32, pack_pair(e[m][2], e[m][3]));
    }
  }
  STAMP(0, 3);
  if (wave >= 4) {
    const int lw = w4i;
    wait_vm<0>();
#pragma unroll
    for (int i = 0; i < 16; ++i) pin(v3[i]);
    if (wb < FF / 32) {
      float s[8];
#pragma unroll
      for (int q = 0; q < 8; ++q) s[q] = 0.f;
#pragma unroll
      for (int i = 0; i < 16; ++i) {
#pragma unroll
        for (int q = 0; q < 4; ++q) { s[2 * q] += bf_lo(v3[i][q]); s[2 * q + 1] += bf_hi(v3[i][q]); }
      }
#pragma unroll
      for (int q = 0; q < 8; ++q) s[q] = grp_sum(ror_add<8>(ror_add<4>(s[q])));
      if (lane < 4) {
        float* dst = (float*)(ws + OFF_CS3) + lw * FF + wb * 32 + lane * 8;
        *(f32x4*)dst = (f32x4){s[0], s[1], s[2], s[3]};
        *(f32x4*)(dst + 4) = (f32x4){s[4], s[5], s[6], s[7]};
      }
    } else if (wb == 127) {
      const int row = (lw < 2) ? lw : SEQ + lw;
      u32x4* dst = (u32x4*)(ws + OFF_H + (size_t)row * DM * 2);
      dst[lane] = (u32x4){0u, 0u, 0u, 0u};
      dst[64 + lane] = (u32x4){0u, 0u, 0u, 0u};
    } else if (wb == 126) {
      u32x4* dst = (u32x4*)(ws + OFF_FLG) + lw * 256 + lane;
#pragma unroll
      for (int i = 0; i < 4; ++i) dst[64 * i] = (u32x4){0u, 0u, 0u, 0u};
    }
  }
}

constexpr u32 T2_WO = 131072, T2_KV = T2_WO + 16384, T2_Z = T2_KV + 2048, T2_G = T2_Z + 512, K2_DUMMY = T2_G + 512;
constexpr int K2_LDS = K2_DUMMY + 256;

__global__ __launch_bounds__(512, 2) void k2_kernel(const u16* __restrict__ x, const u16* __restrict__ wqkv, const u16* __restrict__ wo,
                                                    const u16* __restrict__ w2, const u16* __restrict__ w4, char* __restrict__ ws) {
  extern __shared__ __attribute__((aligned(16))) char smem[];
  const int b = blockIdx.x, xcd = b & 7, idx = b >> 3;
  const int wave = __builtin_amdgcn_readfirstlane(threadIdx.x >> 6), lane = threadIdx.x & 63;
  const int g = lane >> 4, l15 = lane & 15;
  STAMP(1, 0);
  const bool isq = xcd < 4;
  const int rp = ((xcd >> 1) & 1) * 8 + (idx >> 2), cp = (xcd & 1) * 4 + (idx & 3);
  const int w4i = wave & 3, wm = w4i >> 1, wn = w4i & 1;
  const int r0 = wm * 64 + (wave >> 2) * 32 + l15;
  const int row = rp * 128 + r0, col = cp * 128 + wn * 64 + 16 * (g & 1) + 8 * (g >> 1);
  const u32 slot = (u32)(32768 + w4i * 8192 + lane * 16);
  const int mh = xcd * 8 + (idx >> 2), ms = idx & 3;
  auto none = []() {};
  f32x4 e[2][4];
  if (wave >= 4) {
    const int lw = w4i;
    u32 vx[4], vw[4];
    x_voffs<4>(lane, lw, DM, vx);
    const int ldw = isq ? 3072 : DM;
#pragma unroll
    for (int j = 0; j < 4; ++j) {
      int k, c; w256_src(lane, lw * 4 + j, k, c);
      vw[j] = (u32)(k * ldw + cp * 128 + 8 * c) * 2u;
    }
    if (isq) {
      const char* xb0 = (const char*)(x + (size_t)rp * 128 * DM);
      const char* wb0 = (const char*)wqkv;
      auto addr = [&](int t, const char*& xb, const char*& wb) { xb = xb0 + t * 128; wb = wb0 + (size_t)t * (64 * 3072 * 2); };
      f32x4 kp[8], zp;
      auto early = [&]() {
        glds4(T2_G + (lw & 1) * 256, (u32)(rp * 128 + (lw & 1) * 64 + lane) * 4u, ws + OFF_G);
        ld16(zp, ws + OFF_ZP, (u32)((lane >> 3) * 1024 + cp * 128 + lw * 32 + (lane & 7) * 4) * 4u);
      };
      auto side = [&](auto c) {
        if (c.v == 0) {
#pragma unroll
          for (int p = 0; p < 8; ++p) ld16(kp[p], ws + OFF_KVP, (u32)((p * 32 + mh) * 1024 + (lw * 64 + lane) * 4) * 4u);
        } else if (c.v == 1) {
#pragma unroll
          for (int j = 0; j < 4; ++j) {
            int k, cc; w256_src(lane, lw * 2 + (j & 1), k, cc);
            glds16(T2_WO + (u32)((j >> 1) * 8 + lw * 2 + (j & 1)) * 1024u, (u32)((32 * mh + k) * DM + ms * 256 + (j >> 1) * 128 + 8 * cc) * 2u, wo);
          }
        } else {
          pin(zp);
#pragma unroll
          for (int r = 0; r < 4; ++r) zp[r] = grp_sum(ror_add<8>(zp[r]));
          if (lane < 8) *(LAS f32x4*)(T2_Z + (lw * 32 + lane * 4) * 4) = zp;
        }
      };
      loader_loop<4, 4, 4, 16384, 32768, 2, 408, 0>(16, lw, vx, vw, true, true, addr, early, side);
      wait_vm<0>();
#pragma unroll
      for (int p = 0; p < 8; ++p) pin(kp[p]);
      const f32x4 sk = ((kp[0] + kp[1]) + (kp[2] + kp[3])) + ((kp[4] + kp[5]) + (kp[6] + kp[7]));
      *(LAS u32x2*)(T2_KV + (lw * 64 + lane) * 8) = (u32x2){pk2(sk[0], sk[1]), pk2(sk[2], sk[3])};
    } else {
      const char* xb0 = ws + OFF_H + (size_t)(2 + rp * 128) * DM * 2;
      const char* wb0 = (const char*)w2;
      auto addr = [&](int t, const char*& xb, const char*& wb) {
        const int q = t >> 2;
        xb = xb0 + (q & 2 ? 2 : 1) * (q & 1 ? DM * 2 : -DM * 2) + t * 128;
        wb = wb0 + (size_t)t * (64 * DM * 2);
      };
      auto side = [&](auto c) {
        if (c.v == 1) {
#pragma unroll
          for (int j = 0; j < 8; ++j) glds16((u32)(lw * 8 + j) * 1024u, (u32)((row - 32 + (j >> 1) * 16) * DM + col + (j & 1) * 32) * 2u, x);
        } else if (c.v == 2) {
          const u32 u = (u32)(((xcd - 4) * 32 + idx) * 4 + lw) * 2;
          touch(K2_DUMMY, min(u, (u32)(FF * DM * 2 / 8192 - 1)) * 8192 + lane * 128, w4);
          touch(K2_DUMMY, min(u + 1, (u32)(FF * DM * 2 / 8192 - 1)) * 8192 + lane * 128, w4);
        }
      };
      loader_loop<4, 4, 4, 16384, 32768, 0, 20800, 0>(16, lw, vx, vw, true, true, addr, none, side);
      wait_vm<2>();
    }
    wait_lgkm0();
    bar();
    take_rows<4>(e, slot);
  } else {
    f32x4 acc[4][4];
    EXTRA(1, none);
    compute_loop<4, 4, 4, 32768, true, GA44, BI44>(acc, 16, lane, wm * 64, 16384, wn * 4, ex);
    STAMP(1, 2);
    give_rows<4>(acc, e, slot);
    wait_lgkm0();
    bar();
  }
  if (isq) {
    f32x4 zt[4];
#pragma unroll
    for (int n = 0; n < 4; ++n) zt[n] = *(LAS f32x4*)(T2_Z + (wn * 64 + n * 16 + 4 * g) * 4);
    u16* pg = (u16*)(ws + OFF_PG) + (size_t)row * KPG + col;
#pragma unroll
    for (int m = 0; m < 2; ++m) {
#pragma unroll
      for (int hh = 0; hh < 2; ++hh) {
        f32x4 a = e[m][2 * hh], c = e[m][2 * hh + 1];
        float den = 0.f;
#pragma unroll
        for (int r = 0; r < 4; ++r) { a[r] = phi(a[r]); c[r] = phi(c[r]); den += a[r] * zt[2 * hh][r] + c[r] * zt[2 * hh + 1][r]; }
        den = grp_sum(den) + 1e-6f;
        const float sc = *(LAS float*)(T2_G + (r0 + m * 16) * 4) / den;
        st16w(pg + (size_t)m * 16 * KPG + 32 * hh, pack_pair(a * sc, c * sc));
      }
    }
    if (wave < 4) {
      const u32 q = l15 >> 2, p = lane & 3, xm = (q | ((g & 1) << 2)) << 1;
      bf16x8 wf[4];
#pragma unroll
      for (int n = 0; n < 4; ++n) {
        const u32 a = T2_WO + wm * 8192 + (8 * g + q) * 256 + (((u32)(2 * (4 * wn + n)) + (p >> 1)) ^ xm) * 16u + (p & 1) * 8u;
        wf[n] = cat8(lds_tr(a), lds_tr(a + 1024));
      }
      u16* mp = (u16*)(ws + OFF_M) + (size_t)(32 * mh + l15) * DM + ms * 256 + wave * 64 + 16 * (g & 1) + 8 * (g >> 1);
#pragma unroll
      for (int i = 0; i < 2; ++i) {
        const bf16x8 kf = lds_b128(T2_KV + (16 * i + l15) * 64 + g * 16);
        f32x4 mo[4];
#pragma unroll
        for (int n = 0; n < 4; ++n) mo[n] = mfma(wf[n], kf, (f32x4){0.f, 0.f, 0.f, 0.f});
        st16w(mp + (size_t)16 * i * DM, pack_pair(mo[0], mo[1]));
        st16w(mp + (size_t)16 * i * DM + 32, pack_pair(mo[2], mo[3]));
      }
    }
  } else {
    u16* x1 = (u16*)(ws + OFF_X1) + (size_t)row * DM + col;
    f32x2* st = (f32x2*)(ws + OFF_ST) + (size_t)row * 16 + cp * 2 + wn;
#pragma unroll
    for (int m = 0; m < 2; ++m) {
      float v[16];
#pragma unroll
      for (int hh = 0; hh < 2; ++hh) {
        f32x4 a = e[m][2 * hh], c = e[m][2 * hh + 1];
        swap_pair_f32(a, c);
        const u32x4 o = axpy_pack(a, c, 1.0f, *(LAS u32x4*)(u32)((w4i * 8 + (wave >> 2) * 4 + 2 * m + hh) * 1024 + lane * 16));
        st16w(x1 + (size_t)m * 16 * DM + 32 * hh, o);
#pragma unroll
        for (int q = 0; q < 4; ++q) { v[8 * hh + 2 * q] = bf_lo(o[q]); v[8 * hh + 2 * q + 1] = bf_hi(o[q]); }
      }
      float s = 0.f;
#pragma unroll
      for (int q = 0; q < 16; ++q) s += v[q];
      const float mean = grp_sum(s) * (1.0f / 64.0f);
      float q2 = 0.f;
#pragma unroll
      for (int q = 0; q < 16; ++q) q2 += (v[q] - mean) * (v[q] - mean);
      q2 = grp_sum(q2);
      if (g == 0) st[(size_t)m * 16 * 16] = (f32x2){mean, q2};
    }
  }
  STAMP(1, 3);
  wait_vm<0>();
}

constexpr u32 T3_CS = 3 * 49152, T3_RS = T3_CS + 512;
constexpr int K3_LDS = T3_RS + 4096;

__global__ __launch_bounds__(512, 2) void k3_kernel(const u16* __restrict__ w3, char* __restrict__ ws) {
  extern __shared__ __attribute__((aligned(16))) char smem[];
  const int b = blockIdx.x, xcd = b & 7, idx = b >> 3;
  const int wave = __builtin_amdgcn_readfirstlane(threadIdx.x >> 6), lane = threadIdx.x & 63;
  const int g = lane >> 4, l15 = lane & 15;
  STAMP(2, 0);
  const int rp = (xcd >> 2) * 4 + (idx >> 3), cp = (xcd & 3) * 8 + (idx & 7);
  const int w4i = wave & 3;
  const u32 slot = (u32)(49152 + w4i * 14336 + lane * 16);
  f32x4 e[2][7];
  if (wave >= 4) {
    const int lw = w4i;
    u32 vx[8], vw[4];
    x_voffs<8>(lane, lw, DM, vx);
    bool val[2];
#pragma unroll
    for (int j = 0; j < 4; ++j) {
      int k, c; w256_src(lane, lw * 4 + j, k, c);
      vw[j] = (u32)(k * FF + cp * 112 + 8 * c) * 2u;
      val[j >> 1] = c < 14;
    }
    const char* xb0 = ws + OFF_X1 + (size_t)rp * 256 * DM * 2;
    const char* wb0 = (const char*)w3;
    auto addr = [&](int t, const char*& xb, const char*& wb) { xb = xb0 + t * 128; wb = wb0 + (size_t)t * (64 * FF * 2); };
    f32x4 sa[4], sc[4], cq;
    float gt[4];
    const int row = rp * 256 + lw * 64 + l15;
    auto none = []() {};
    auto side = [&](auto c) {
#pragma unroll
      for (int m = 2 * c.v; m < 2 * c.v + 2; ++m) {
        ld16(sa[m], ws + OFF_ST, (u32)((row + m * 16) * 128 + g * 32));
        ld16(sc[m], ws + OFF_ST, (u32)((row + m * 16) * 128 + g * 32 + 16));
        ld4(gt[m], ws + OFF_G, (u32)(row + m * 16) * 4u);
      }
      if (c.v == 0) ld16(cq, ws + OFF_CS3, (u32)(g * FF + cp * 112 + min(lw * 8 + (l15 & 7), 27) * 4) * 4u);
    };
    loader_loop<8, 4, 3, 32768, 49152, 0, 607, 0>(16, lw, vx, vw, val[0], val[1], addr, none, side);
    wait_vm<6>();
    pin(cq);
#pragma unroll
    for (int r = 0; r < 4; ++r) cq[r] = grp_sum(cq[r]);
    if (lane < 8) *(LAS f32x4*)(T3_CS + (lw * 8 + lane) * 16) = cq;
#pragma unroll
    for (int m = 0; m < 4; ++m) {
      if (m == 2) wait_vm<0>();
      pin(sa[m]); pin(sc[m]); pin(gt[m]);
      const f32x4 a = sa[m], d = sc[m];
      const float mean = grp_sum((a[0] + a[2]) + (d[0] + d[2])) * (1.0f / 16.0f);
      const float d0 = a[0] - mean, d1 = a[2] - mean, d2 = d[0] - mean, d3 = d[2] - mean;
      const float m2 = grp_sum((a[1] + a[3]) + (d[1] + d[3]) + 64.0f * ((d0 * d0 + d1 * d1) + (d2 * d2 + d3 * d3)));
      const float rs = __builtin_amdgcn_rsqf(m2 * (1.0f / DM) + 1e-5f);
      if (g == 0) *(LAS f32x4*)(T3_RS + (lw * 64 + m * 16 + l15) * 16) = (f32x4){rs, mean * rs, 1.0f - gt[m], 0.f};
    }
    wait_lgkm0();
    bar();
    take_rows<7>(e, slot);
  } else {
    f32x4 acc[4][7];
    auto none = []() {};
    EXTRA(2, none);
    compute_loop<4, 7, 3, 49152, true, GA47, BI47>(acc, 16, lane, wave * 64, 32768, 0, ex);
    STAMP(2, 2);
    give_rows<7>(acc, e, slot);
    wait_lgkm0();
    bar();
  }
  const int r0 = w4i * 64 + (wave >> 2) * 32 + l15;
  f32x4 cs[7];
#pragma unroll
  for (int n = 0; n < 7; ++n) cs[n] = *(LAS f32x4*)(T3_CS + (n * 16 + 4 * g) * 4);
  u16* pg = (u16*)(ws + OFF_PG) + (size_t)(rp * 256 + r0) * KPG + DM + cp * 112;
  const int co = 16 * (g & 1) + 8 * (g >> 1);
#pragma unroll
  for (int m = 0; m < 2; ++m) {
    const f32x4 rs = *(LAS f32x4*)(T3_RS + (r0 + m * 16) * 16);
#pragma unroll
    for (int n = 0; n < 7; ++n)
#pragma unroll
      for (int r = 0; r < 4; ++r) e[m][n][r] = rs[2] * gelu_tanh(e[m][n][r] * rs[0] - rs[1] * cs[n][r]);
    u16* o = pg + (size_t)m * 16 * KPG;
#pragma unroll
    for (int p = 0; p < 3; ++p) st16w(o + 32 * p + co, pack_pair(e[m][2 * p], e[m][2 * p + 1]));
    st8w(o + 96 + 4 * g, (u32x2){pk2(e[m][6][0], e[m][6][1]), pk2(e[m][6][2], e[m][6][3])});
  }
  STAMP(2, 3);
}

constexpr u32 T4_G = 4 * 32768;
constexpr int K4_LDS = T4_G + 512;
constexpr int K4_NT = KPG / 64;

__global__ __launch_bounds__(512, 2) void k4_kernel(const u16* __restrict__ w4, char* __restrict__ ws, u16* __restrict__ out) {
  extern __shared__ __attribute__((aligned(16))) char smem[];
  const int b = blockIdx.x, xcd = b & 7, idx = b >> 3;
  const bool first = xcd < 4;
  const int wave = __builtin_amdgcn_readfirstlane(threadIdx.x >> 6), lane = threadIdx.x & 63;
  const int g = lane >> 4, l15 = lane & 15;
  STAMP(3, 0);
  const int rp = ((xcd >> 1) & 1) * 8 + (idx >> 2), cp = (xcd & 1) * 4 + (idx & 3), tile = rp * 8 + cp;
  const int t0 = first ? 0 : K4_NH, nt = first ? K4_NH : K4_NT - K4_NH;
  const int w4i = wave & 3;
  GAS u32* flag = (GAS u32*)(ws + OFF_FLG + tile * 128);
  char* part = ws + OFF_PRT + (size_t)(tile * 4 + w4i) * 8192;
  const u32 slot0 = (u32)(((nt & 3) * 4 + w4i) * 8192), slot = slot0 + lane * 16;
  const int wm = w4i >> 1, wn = w4i & 1;
  const int row = rp * 128 + wm * 64 + l15, col = cp * 128 + wn * 64 + 16 * (g & 1) + 8 * (g >> 1);
  if (wave >= 4) {
    const int lw = w4i;
    u32 vx[4], vw[4];
    x_voffs<4>(lane, lw, KPG, vx);
#pragma unroll
    for (int j = 0; j < 4; ++j) {
      int k, c; w256_src(lane, lw * 4 + j, k, c);
      vw[j] = (u32)(k * DM + cp * 128 + 8 * c) * 2u;
    }
    const char* xb0 = ws + OFF_PG + (size_t)rp * 128 * KPG * 2 + t0 * 128;
    const char* wm0 = ws + OFF_M;
    const char* w40 = (const char*)w4 - (size_t)DM * DM * 2;
    auto addr = [&](int t, const char*& xb, const char*& wb) { xb = xb0 + t * 128; wb = (t + t0 < 16 ? wm0 : w40) + (size_t)(t + t0) * (64 * DM * 2); };
    auto none = []() {};
    if (first) {
      glds4(T4_G + (lw & 1) * 256, (u32)(rp * 128 + (lw & 1) * 64 + lane) * 4u, ws + OFF_G);
      auto side = [&](auto c) {
        if (c.v == 1) {
#pragma unroll
          for (int j = 0; j < 8; ++j) glds16(slot0 + j * 1024u, (u32)((row + (j >> 1) * 16) * DM + col + (j & 1) * 32) * 2u, ws + OFF_X1);
        }
      };
      loader_loop<4, 4, 4, 16384, 32768, 0, 800, 0>(nt, lw, vx, vw, true, true, addr, none, side);
      wait_vm<0>();
      bar();
      return;
    }
    auto side = [&](auto) {};
    loader_loop<4, 4, 4, 16384, 32768, 0, 0, 1>(nt, lw, vx, vw, true, true, addr, none, side);
    const bool ready = __builtin_amdgcn_readfirstlane(__hip_atomic_load(flag, __ATOMIC_RELAXED, __HIP_MEMORY_SCOPE_AGENT)) == 1u;
    wait_vm<0>();
    if (!ready) {
      bar();
      while (__hip_atomic_load(flag, __ATOMIC_RELAXED, __HIP_MEMORY_SCOPE_AGENT) != 1u) __builtin_amdgcn_s_sleep(1);
    }
    asm volatile("" ::: "memory");
    u32x4 pv[8];
#pragma unroll
    for (int j = 0; j < 8; ++j) ld16c(pv[j], part, (u32)(j * 1024 + lane * 16));
    if (ready) bar();
    wait_vm<0>();
#pragma unroll
    for (int j = 0; j < 8; ++j) { pin(pv[j]); *(LAS u32x4*)(slot + j * 1024) = pv[j]; }
    wait_lgkm0();
    bar();
    return;
  }
  f32x4 acc[4][4];
  auto none = []() {};
  EXTRA(3, none);
  compute_loop<4, 4, 4, 32768, true, GA44, BI44>(acc, nt, lane, wm * 64, 16384, wn * 4, ex);
  STAMP(3, 2);
  bar();
#pragma unroll
  for (int m = 0; m < 4; ++m) {
    const float og = first ? 1.0f - *(LAS float*)(T4_G + (wm * 64 + m * 16 + l15) * 4) : 1.0f;
#pragma unroll
    for (int hh = 0; hh < 2; ++hh) {
      f32x4 a = acc[m][2 * hh], c = acc[m][2 * hh + 1];
      swap_pair_f32(a, c);
      const u32x4 o = axpy_pack(a, c, og, *(LAS u32x4*)(slot + (2 * m + hh) * 1024));
      if (first) st16c(part, (u32)((2 * m + hh) * 1024 + lane * 16), o);
      else st16w(out + (size_t)(row + m * 16) * DM + col + 32 * hh, o);
    }
  }
  if (first) {
    wait_vm<0>();
    bar();
    if (threadIdx.x == 0) __hip_atomic_store(flag, 1u, __ATOMIC_RELAXED, __HIP_MEMORY_SCOPE_AGENT);
  }
  STAMP(3, 3);
}

}

extern "C" void kernel_launch(void* const* d_in, const int* in_sizes, int n_in, void* d_out, int out_size, void* d_ws, size_t ws_size,
                              hipStream_t stream) {
  const u16* x = (const u16*)d_in[0];
  const u16* wqkv = (const u16*)d_in[1];
  const u16* wo = (const u16*)d_in[2];
  const u16* w1 = (const u16*)d_in[4];
  const u16* w2 = (const u16*)d_in[5];
  const u16* w3 = (const u16*)d_in[6];
  const u16* w4 = (const u16*)d_in[7];
  const u16* gw = (const u16*)d_in[8];
  char* ws = (char*)d_ws;
  (void)hipFuncSetAttribute((const void*)k1_kernel, hipFuncAttributeMaxDynamicSharedMemorySize, K1_LDS);
  (void)hipFuncSetAttribute((const void*)k2_kernel, hipFuncAttributeMaxDynamicSharedMemorySize, K2_LDS);
  (void)hipFuncSetAttribute((const void*)k3_kernel, hipFuncAttributeMaxDynamicSharedMemorySize, K3_LDS);
  (void)hipFuncSetAttribute((const void*)k4_kernel, hipFuncAttributeMaxDynamicSharedMemorySize, K4_LDS);
  k1_kernel<<<256, 512, K1_LDS, stream>>>(x, wqkv, w1, w3, gw, wo, w2, ws);
  k2_kernel<<<256, 512, K2_LDS, stream>>>(x, wqkv, wo, w2, w4, ws);
  k3_kernel<<<256, 512, K3_LDS, stream>>>(w3, ws);
  k4_kernel<<<256, 512, K4_LDS, stream>>>(w4, ws, (u16*)d_out);
}
```

```cpp
#include <hip/hip_runtime.h>
#include <hip/hip_bf16.h>
#include <stdint.h>

namespace {

typedef unsigned short u16;
typedef unsigned int u32;
typedef short s16x4 __attribute__((ext_vector_type(4)));
typedef short s16x8 __attribute__((ext_vector_type(8)));
typedef __bf16 bf16x8 __attribute__((ext_vector_type(8)));
typedef __bf16 bf16x2 __attribute__((ext_vector_type(2)));
typedef float f32x2 __attribute__((ext_vector_type(2)));
typedef float f32x4 __attribute__((ext_vector_type(4)));
typedef u32 u32x2 __attribute__((ext_vector_type(2)));
typedef u32 u32x4 __attribute__((ext_vector_type(4)));
#define LAS __attribute__((address_space(3)))
#define GAS __attribute__((address_space(1)))
#define DI __device__ __forceinline__

constexpr int SEQ = 2048, DM = 1024, FF = 3584, KPG = DM + FF;

constexpr size_t OFF_H   = 0;
constexpr size_t OFF_X1  = OFF_H + (size_t)(SEQ + 32) * DM * 2;
constexpr size_t OFF_PG  = OFF_X1 + (size_t)SEQ * DM * 2;
constexpr size_t OFF_M   = OFF_PG + (size_t)SEQ * KPG * 2;
constexpr size_t OFF_KVP = OFF_M + (size_t)DM * DM * 2;
constexpr size_t OFF_ZP  = OFF_KVP + (size_t)16 * 32 * 1024 * 4;
constexpr size_t OFF_G   = OFF_ZP + (size_t)16 * 32 * 32 * 4;
constexpr size_t OFF_ST  = OFF_G + (size_t)SEQ * 4;
constexpr size_t OFF_CS3 = OFF_ST + (size_t)SEQ * 16 * 8;
constexpr size_t OFF_FLG = OFF_CS3 + (size_t)16 * FF * 4;
constexpr size_t OFF_FG  = OFF_FLG + 8192;
constexpr size_t OFF_FX  = OFF_FG + 1024;
constexpr size_t OFF_FQ  = OFF_FX + 1024;
constexpr size_t OFF_FKV = OFF_FQ + 512;
constexpr size_t OFF_BA  = OFF_FG + 4096;
constexpr size_t OFF_FH  = OFF_BA + 1024;
constexpr size_t OFF_BQ  = OFF_FH + 4096;
constexpr size_t OFF_BR  = OFF_BQ + 1024;
constexpr size_t OFF_PRT = OFF_BR + 1024;

#define GA84 24
#define BI84 4
#define GA44 12
#define BI44 4

DI u32 pk2(float a, float b) { bf16x2 v = __builtin_convertvector((f32x2){a, b}, bf16x2); return __builtin_bit_cast(u32, v); }
DI float bf_lo(u32 u) { return __uint_as_float(u << 16); }
DI float bf_hi(u32 u) { return __uint_as_float(u & 0xffff0000u); }
DI bf16x8 lds_b128(u32 a) { return *(const LAS bf16x8*)a; }
DI s16x4 lds_tr(u32 a) { return __builtin_amdgcn_ds_read_tr16_b64_v4i16((LAS s16x4*)a); }
DI bf16x8 cat8(s16x4 a, s16x4 b) { return __builtin_bit_cast(bf16x8, __builtin_shufflevector(a, b, 0, 1, 2, 3, 4, 5, 6, 7)); }
DI f32x4 mfma(bf16x8 a, bf16x8 b, f32x4 c) { return __builtin_amdgcn_mfma_f32_16x16x32_bf16(a, b, c, 0, 0, 0); }
DI void bar() { asm volatile("" ::: "memory"); __builtin_amdgcn_s_barrier(); asm volatile("" ::: "memory"); }
DI void wait_lgkm0() { asm volatile("" ::: "memory"); __builtin_amdgcn_s_waitcnt(0xC07F); asm volatile("" ::: "memory"); }
template <int N> DI void wait_vm() { asm volatile("s_waitcnt vmcnt(%0)" ::"n"(N) : "memory"); }
#define SCHED() __builtin_amdgcn_sched_barrier(0)

DI void glds16(u32 lds, u32 voff, const void* gbase) {
  u32 keep;
  asm volatile("s_mov_b32 %0, m0\n\ts_mov_b32 m0, %1\n\ts_nop 3\n\tglobal_load_lds_dwordx4 %2, %3\n\ts_mov_b32 m0, %0"
               : "=&s"(keep) : "s"(__builtin_amdgcn_readfirstlane(lds)), "v"(voff), "s"(gbase) : "memory");
}
DI void glds4(u32 lds, u32 voff, const void* gbase) {
  u32 keep;
  asm volatile("s_mov_b32 %0, m0\n\ts_mov_b32 m0, %1\n\ts_nop 3\n\tglobal_load_lds_dword %2, %3\n\ts_mov_b32 m0, %0"
               : "=&s"(keep) : "s"(__builtin_amdgcn_readfirstlane(lds)), "v"(voff), "s"(gbase) : "memory");
}
DI void touch(u32 lds, u32 voff, const void* gbase) { glds4(lds, voff, gbase); }
#define CODE_TOUCH(label, lds, first) do { unsigned long long pc_; int len_; \
    asm volatile("s_getpc_b64 %0\n\ts_mov_b32 %1, " label "@rel32@lo+4" : "=s"(pc_), "=s"(len_)); \
    touch(lds, (u32)max(min((first) * 8192 + lane * 128, len_), 0) & ~3u, (const void*)pc_); } while (0)
#define CODE_MARK(label) asm volatile(label ":")
template <class T> DI void ld16(T& v, const void* base, u32 voff) { asm volatile("global_load_dwordx4 %0, %1, %2" : "=v"(v) : "v"(voff), "s"(base) : "memory"); }
template <class T> DI void ld16c(T& v, const void* base, u32 voff) { asm volatile("global_load_dwordx4 %0, %1, %2 sc1" : "=v"(v) : "v"(voff), "s"(base) : "memory"); }
DI void st16c(void* base, u32 voff, u32x4 v) { asm volatile("global_store_dwordx4 %0, %1, %2 sc1\n\ts_nop 1" ::"v"(voff), "v"(v), "s"(base) : "memory"); }
DI void st16w(void* p, u32x4 v) {
  *(u32x4*)p = v;
}
template <class T> DI void pin(T& v) { asm volatile("; pin %0" : "+v"(v)); }
DI void stg16(void* p, u32x4 v) { asm volatile("global_store_dwordx4 %0, %1, off sc1\n\ts_nop 1" ::"v"(p), "v"(v) : "memory"); }
DI void stg8(void* p, u32x2 v) { asm volatile("global_store_dwordx2 %0, %1, off sc1\n\ts_nop 1" ::"v"(p), "v"(v) : "memory"); }
DI void st8c(void* base, u32 voff, u32x2 v) { asm volatile("global_store_dwordx2 %0, %1, %2 sc1\n\ts_nop 1" ::"v"(voff), "v"(v), "s"(base) : "memory"); }
DI void ld8(f32x2& v, const void* base, u32 voff) { asm volatile("global_load_dwordx2 %0, %1, %2" : "=v"(v) : "v"(voff), "s"(base) : "memory"); }
DI void ld8c(u32x2& v, const void* base, u32 voff) { asm volatile("global_load_dwordx2 %0, %1, %2 sc1" : "=v"(v) : "v"(voff), "s"(base) : "memory"); }
DI void ld4c(u32& v, const void* base, u32 voff) { asm volatile("global_load_dword %0, %1, %2 sc1" : "=v"(v) : "v"(voff), "s"(base) : "memory"); }
DI void set_flag(GAS u32* f) { __hip_atomic_store(f, 1u, __ATOMIC_RELAXED, __HIP_MEMORY_SCOPE_AGENT); }
template <int SL = 1> DI void wait_flags(GAS u32* f) {
  int spins = 0;
  while (!__all(__hip_atomic_load(f, __ATOMIC_RELAXED, __HIP_MEMORY_SCOPE_AGENT) == 1u) && ++spins < (1 << 22)) __builtin_amdgcn_s_sleep(SL);
}
DI int fresh_lane() { int l; asm volatile("v_mbcnt_lo_u32_b32 %0, -1, 0" : "=v"(l)); asm volatile("v_mbcnt_hi_u32_b32 %0, -1, %0" : "+v"(l)); return l; }
DI void stg4(void* p, u32 v) { asm volatile("global_store_dword %0, %1, off sc1\n\ts_nop 1" ::"v"(p), "v"(v) : "memory"); }
DI void ld4fc(float& v, const void* base, u32 voff) { asm volatile("global_load_dword %0, %1, %2 sc1" : "=v"(v) : "v"(voff), "s"(base) : "memory"); }

DI float grp_sum(float v) {
  u32 u = __float_as_uint(v);
  auto r = __builtin_amdgcn_permlane16_swap(u, u, false, false);
  float s = __uint_as_float(r[0]) + __uint_as_float(r[1]);
  u = __float_as_uint(s);
  auto q = __builtin_amdgcn_permlane32_swap(u, u, false, false);
  return __uint_as_float(q[0]) + __uint_as_float(q[1]);
}
template <int N> DI float ror_add(float v) {
  return v + __uint_as_float(__builtin_amdgcn_update_dpp(0u, __float_as_uint(v), 0x120 + N, 0xf, 0xf, false));
}

DI f32x4 gelu_consts(float mean, float r, float inv_rs) {
  const float c0 = -2.0f * 0.7978845608028654f * 1.4426950408889634f, c1 = c0 * 0.044715f;
  return (f32x4){-mean, c0 * r, c1 * r * r * r, inv_rs};
}
DI f32x2 ln_gelu2(f32x2 e, const f32x4& k, f32x2 cs) {
  const f32x2 x = cs * k[0] + e;
  const f32x2 u = x * ((x * x) * k[2] + k[1]);
  const f32x2 d = (f32x2){__builtin_amdgcn_exp2f(u[0]), __builtin_amdgcn_exp2f(u[1])} * k[3] + k[3];
  return x * (f32x2){__builtin_amdgcn_rcpf(d[0]), __builtin_amdgcn_rcpf(d[1])};
}
DI f32x4 ln_gelu4(f32x4 e, const f32x4& k, f32x4 cs) {
  const f32x2 a = ln_gelu2(e.lo, k, cs.lo), b = ln_gelu2(e.hi, k, cs.hi);
  return (f32x4){a[0], a[1], b[0], b[1]};
}
DI float phi(float t) { return (t > 0.0f ? t + 1.0f : __builtin_amdgcn_exp2f(t * 1.4426950408889634f)) + 1e-6f; }

DI void swap_pair_f32(f32x4& a, f32x4& b) {
#pragma unroll
  for (int r = 0; r < 4; ++r) {
    auto s = __builtin_amdgcn_permlane16_swap(__float_as_uint(a[r]), __float_as_uint(b[r]), false, false);
    a[r] = __uint_as_float(s[0]); b[r] = __uint_as_float(s[1]);
  }
}
DI u32x4 pack_pair(const f32x4& a, const f32x4& b) {
  auto s0 = __builtin_amdgcn_permlane16_swap(pk2(a[0], a[1]), pk2(b[0], b[1]), false, false);
  auto s1 = __builtin_amdgcn_permlane16_swap(pk2(a[2], a[3]), pk2(b[2], b[3]), false, false);
  return (u32x4){s0[0], s1[0], s0[1], s1[1]};
}
DI u32x4 axpy_pack(f32x4 a, f32x4 c, float s, u32x4 x) {
  return (u32x4){pk2(a[0] + s * bf_lo(x[0]), a[1] + s * bf_hi(x[0])), pk2(a[2] + s * bf_lo(x[1]), a[3] + s * bf_hi(x[1])),
                 pk2(c[0] + s * bf_lo(x[2]), c[1] + s * bf_hi(x[2])), pk2(c[2] + s * bf_lo(x[3]), c[3] + s * bf_hi(x[3]))};
}

template <int I> struct IC { static constexpr int v = I; };
template <int P, int EX, int R, int S, int NCS, int LAST, class Side> DI void loader_tail(const Side& side) {
  constexpr int c = S - 2 - R;
  side(IC<c>{});
  if constexpr (R >= LAST) {
    wait_vm<P * R + EX + NCS % 100 + (c >= 1 ? NCS / 100 % 100 : 0) + (c >= 2 ? NCS / 10000 : 0)>();
    bar();
    if constexpr (R > 0) loader_tail<P, EX, R - 1, S, NCS, LAST>(side);
  }
}
struct NoEach { enum { EX = 0, PRE = 2 }; DI void operator()(int) const {} };
template <int NPX, int NPW, int XBYTES, int STAGE, class Addr>
DI void loader_issue(int t, int st, int lw, const u32 (&vx)[NPX], const u32 (&vw)[NPW], const Addr& addr) {
  const char *xb, *wb;
  addr(t, xb, wb);
  const u32 base = (u32)st * STAGE;
#pragma unroll
  for (int j = 0; j < NPX; ++j) glds16(base + (u32)(lw * NPX + j) * 1024u, vx[j], xb);
#pragma unroll
  for (int j = 0; j < NPW; ++j) glds16(base + XBYTES + (u32)(lw * NPW + j) * 1024u, vw[j], wb);
}
template <int NPX, int NPW, int S, int XBYTES, int STAGE, int NCS, int ST0, bool START, class Addr, class Side, class Each = NoEach>
DI void loader_loop(int NT, int lw, const u32 (&vx)[NPX], const u32 (&vw)[NPW], const Addr& addr, const Side& side, const Each& each = Each()) {
  constexpr int EX = (int)Each::EX, P = NPX + NPW + EX, PRE = (int)Each::PRE < S ? (int)Each::PRE : S;
  auto issue = [&](int t, int st) { loader_issue<NPX, NPW, XBYTES, STAGE>(t, st, lw, vx, vw, addr); each(t); };
  if constexpr (START) {
#pragma unroll
    for (int t = 0; t < PRE; ++t) issue(t, (t + ST0) % S);
    wait_vm<P*(PRE - 1) + EX>();
    bar();
  }
#pragma unroll
  for (int t = PRE; t < S; ++t) issue(t, (t + ST0) % S);
  int st = ST0;
#pragma unroll 1
  for (int t = 1; t <= NT - S; ++t) {
    wait_vm<P*(S - 2) + EX>();
    bar();
    issue(t - 1 + S, st);
    st = (st + 1 == S) ? 0 : st + 1;
  }
  loader_tail<P, EX, S - 2, S, NCS, 0>(side);
}

struct NoExtra {
  static constexpr int N0 = 0, N1 = 0;
  template <int KS, int I, class F> DI void step(const F&) {}
  DI void pre_bar() {}
};
template <int WM_T, int WN_T> struct Frags {
  bf16x8 x[WM_T];
  s16x4 lo[WN_T], hi[WN_T];
  DI bf16x8 w(int n) const { return cat8(lo[n], hi[n]); }
};
template <int WN_T> struct Addrs { u32 a[WN_T + 2]; };

template <int R, int KS, int WM_T, int WN_T>
DI void rd_one(Frags<WM_T, WN_T>& f, const Addrs<WN_T>& ad) {
  if constexpr (R == 0) f.x[0] = lds_b128(ad.a[KS]);
  else if constexpr (R <= 2 * WN_T) {
    constexpr int n = (R - 1) >> 1;
    if constexpr ((R - 1) & 1) f.hi[n] = lds_tr(ad.a[2 + n] + (KS * 32 + 4) * 256); else f.lo[n] = lds_tr(ad.a[2 + n] + KS * 32 * 256);
  } else if constexpr (R < WM_T + 2 * WN_T) f.x[R - 2 * WN_T] = lds_b128(ad.a[KS] + (R - 2 * WN_T) * 2048);
}
template <int R, int RE, int KS, int WM_T, int WN_T>
DI void rd_range(Frags<WM_T, WN_T>& f, const Addrs<WN_T>& ad) {
  if constexpr (R < RE) { rd_one<R, KS, WM_T, WN_T>(f, ad); rd_range<R + 1, RE, KS, WM_T, WN_T>(f, ad); }
}
template <int R, int RE, int WN_T> DI void adv_range(Addrs<WN_T>& ad, u32 d) {
  if constexpr (R < RE) { ad.a[R] += d; pin(ad.a[R]); adv_range<R + 1, RE>(ad, d); }
}

template <int I, int KS, int WM_T, int WN_T, bool ORIENT_T, int G0, int G1, bool MORE, class Extra>
DI void sub_step(f32x4 (&acc)[WM_T][WN_T], const Frags<WM_T, WN_T>& c, Frags<WM_T, WN_T>& n, Addrs<WN_T>& ad, u32 d, Extra& ex) {
  constexpr int NM = WM_T * WN_T, NS = NM + (KS ? Extra::N1 : Extra::N0), NR = WM_T + 2 * WN_T;
  if constexpr (I < NS) {
    if constexpr (KS == 1 && MORE && I == G0) { ex.pre_bar(); wait_lgkm0(); bar(); }
    if constexpr (I < NM) {
      constexpr int m = I / WN_T, j = I % WN_T;
      acc[m][j] = ORIENT_T ? mfma(c.w(j), c.x[m], acc[m][j]) : mfma(c.x[m], c.w(j), acc[m][j]);
    } else {
      ex.template step<KS, I - NM>(c);
    }
    if constexpr ((KS == 0 || MORE) && I >= G0 && I < G1) {
      SCHED();
      rd_range<(I - G0) * NR / (G1 - G0), (I + 1 - G0) * NR / (G1 - G0), 1 - KS, WM_T, WN_T>(n, ad);
    }
    if constexpr (KS == 0 && MORE && I >= G1) adv_range<(I - G1) * (WN_T + 2) / (NS - G1), (I + 1 - G1) * (WN_T + 2) / (NS - G1)>(ad, d);
    SCHED();
    sub_step<I + 1, KS, WM_T, WN_T, ORIENT_T, G0, G1, MORE>(acc, c, n, ad, d, ex);
  }
}

template <int WM_T, int WN_T, int S, int STAGE, bool ORIENT_T, int G1A, int BARI, int ST0 = 0, class Extra>
DI void compute_loop(f32x4 (&acc)[WM_T][WN_T], int NT, int lane, int xrow0, u32 wbase, int nt0, Extra& ex) {
  constexpr int NM = WM_T * WN_T;
  Frags<WM_T, WN_T> F0, F1;
  Addrs<WN_T> ad;
  {
    const u32 g = lane >> 4, f = lane & 7, r = (u32)(xrow0 + (lane & 15)) * 128u;
    ad.a[0] = r + ((g ^ f) << 4);
    ad.a[1] = r + (((4 + g) ^ f) << 4);
    const u32 q = (lane & 15) >> 2, p = lane & 3, xm = (q | ((g & 1) << 2)) << 1;
#pragma unroll
    for (int n = 0; n < WN_T; ++n) ad.a[2 + n] = wbase + (8 * g + q) * 256 + (((u32)(2 * (nt0 + n)) + (p >> 1)) ^ xm) * 16u + (p & 1) * 8u;
  }
  adv_range<0, WN_T + 2>(ad, (u32)(ST0 * STAGE));
#pragma unroll
  for (int m = 0; m < WM_T; ++m)
#pragma unroll
    for (int n = 0; n < WN_T; ++n) { acc[m][n] = (f32x4){0.f, 0.f, 0.f, 0.f}; pin(acc[m][n]); }
  wait_lgkm0();
  bar();
  ex.start();
  rd_range<0, WM_T + 2 * WN_T, 0, WM_T, WN_T>(F0, ad);
  int st = ST0 + 1;
  for (int t = 0; t < NT - 1; ++t) {
    const u32 d = (st == S) ? (u32)(-(S - 1) * STAGE) : (u32)STAGE;
    st = (st == S) ? 1 : st + 1;
    sub_step<0, 0, WM_T, WN_T, ORIENT_T, 0, G1A, true>(acc, F0, F1, ad, d, ex);
    sub_step<0, 1, WM_T, WN_T, ORIENT_T, BARI, NM + Extra::N1, true>(acc, F1, F0, ad, d, ex);
  }
  sub_step<0, 0, WM_T, WN_T, ORIENT_T, 0, G1A, false>(acc, F0, F1, ad, 0u, ex);
  sub_step<0, 1, WM_T, WN_T, ORIENT_T, 0, 1, false>(acc, F1, F0, ad, 0u, ex);
}
template <class F> struct StartExtra : NoExtra {
  const F& f;
  DI void start() { f(); }
};
struct Boot {
  char* ws; int b, state; u32 n;
  DI void look() { asm volatile("global_load_dword v250, %0, %1 sc1" ::"v"(0u), "s"(ws + OFF_BR + b * 4) : "memory", "v250"); }
  DI void step() {
    if (state == 2) return;
    u32 v;
    asm volatile("v_readfirstlane_b32 %0, v250" : "=s"(v));
    if (state == 1 && v == n) {
      state = 2;
      if (fresh_lane() == 0) set_flag((GAS u32*)(ws + OFF_BA) + b);
      return;
    }
    if (state == 0) {
      n = v + 1u; state = 1;
      if (fresh_lane() == 0) __hip_atomic_store((GAS u32*)(ws + OFF_BQ) + b, n, __ATOMIC_RELAXED, __HIP_MEMORY_SCOPE_AGENT);
    }
    look();
  }
  DI void finish() {
#pragma nounroll
    for (int spins = 0; state != 2 && spins < (1 << 22); ++spins) { wait_vm<0>(); step(); }
  }
};
struct BootMaster {
  char* ws; u32 o16; int state;
  DI void begin() {
#pragma unroll
    for (int i = 0; i < 17; ++i) st16c(ws + OFF_FLG + i * 1024, o16, (u32x4){0u, 0u, 0u, 0u});
  }
  DI void step() {
    if (state == 2) return;
    if (state == 1) {
      u32 a;
      asm volatile("v_and_b32 %0, v244, v245\n\tv_and_b32 %0, %0, v246\n\tv_and_b32 %0, %0, v247" : "=v"(a));
      if (__all(a == 1u)) { state = 2; return; }
      asm volatile("global_store_dwordx4 %0, v[240:243], %1 sc1\n\ts_nop 1" ::"v"(o16), "s"(ws + OFF_BR) : "memory");
    }
    state = 1;
    asm volatile("global_load_dwordx4 v[240:243], %0, %1 sc1\n\tglobal_load_dwordx4 v[244:247], %0, %2 sc1" ::"v"(o16), "s"(ws + OFF_BQ), "s"(ws + OFF_BA)
                 : "memory", "v240", "v241", "v242", "v243", "v244", "v245", "v246", "v247");
  }
  DI void finish() {
#pragma nounroll
    for (int spins = 0; state != 2 && spins < (1 << 22); ++spins) { wait_vm<0>(); step(); }
  }
};
template <int PRE_, class F> struct EachFn { enum { EX = 0, PRE = PRE_ }; const F& f; DI void operator()(int t) const { f(t); } };
#define EXTRA(kid_, f_) StartExtra<decltype(f_)> ex{{}, f_}

template <int OFF, class T> DI void ldx(T& v, const void* base, u32 voff) {
  asm volatile("global_load_dwordx4 %0, %1, %2 offset:%3 sc1" : "=v"(v) : "v"(voff), "s"(base), "n"(OFF) : "memory");
}
template <int WM, int WN, int XD, int WR> struct DX {
  f32x4 acc[WM][WN];
  bf16x8 x[XD][2][WM];
  s16x4 lo[WR], hi[WR];
  u32 a[WN], vx[WM == 2 ? 4 : WM];
};
template <int WM_, int WN_, int XD_, int NTB_, bool LAST_> struct Plain {
  static constexpr int WM = WM_, WN = WN_, XD = XD_, NTB = NTB_, DRAIN = -1, PRE = 0;
  static constexpr bool LAST = LAST_;
  static constexpr int nld(int Q) { return Q % WN < WM && (!LAST || Q / WN - 1 + 2 * XD < 2 * NTB); }
  static constexpr int nop(int) { return 0; }
  static constexpr int nbar(int) { return 0; }
  static constexpr bool xbar(int) { return false; }
};
template <class S, int WR> struct Count {
  static constexpr int NF = 2 * S::WN, L0 = (2 * S::XD - 1) * S::WM;
  static constexpr int cum(int Q) { int c = L0; for (int q = 0; q < Q; ++q) c += S::nld(q); return c; }
  static constexpr int nop0(int q0, int q1) { int c = 0; for (int q = q0; q < q1; ++q) c += S::nop(q); return c; }
  static constexpr int head(int q) { return (q + WR - 1) % NF == 0 ? S::nbar((q + WR - 1) / NF) : 0; }
  static constexpr int behind_load(int idx, int Q) {
    int c = cum(Q) - 1 - idx + (idx < L0 ? S::PRE : 0);
    for (int q = 0; q <= Q; ++q) if (cum(q) > idx) c += head(q) + (q < Q ? S::nop(q) : 0);
    return c;
  }
  static constexpr int behind_ops(int Q) {
    int last = 0;
    for (int q = 0; q < Q; ++q) if (S::nop(q)) last = q;
    return cum(Q) - cum(last);
  }
};
template <class S, int I, int N, int WM, int WN, int XD, int WR, class XA>
DI void dx_loads(DX<WM, WN, XD, WR>& s, const XA& xaddr, int t0) {
  if constexpr (N > 0) {
    constexpr int h = I / WM, m = I % WM;
    ldx<(h & 1) * 1024>(s.x[(h >> 1) % XD][h & 1][m], xaddr(t0 + (h >> 1)), s.vx[WM == 2 ? (h >> 3) * 2 + m : m]);
    dx_loads<S, I + 1, N - 1>(s, xaddr, t0);
  }
}
template <class S, int Q, int WM, int WN, int XD, int WR, class XA, class Hook>
DI void dx_step(DX<WM, WN, XD, WR>& s, const XA& xaddr, int t0, Hook& hook) {
  using C = Count<S, WR>;
  constexpr int NF = 2 * WN, NQ = S::NTB * NF;
  if constexpr (Q < NQ) {
    constexpr int u = Q / NF, f = Q % NF, ks = f / WN, n = f % WN, j = 2 * u + ks;
    constexpr int QN = Q + WR - 1, un = (QN / NF) & 3, fn = QN % NF, ksn = fn / WN, nn = fn % WN;
    constexpr bool rd = !S::LAST || QN < NQ;
    if constexpr (Q == S::DRAIN) wait_vm<C::cum(Q) - C::L0>();
    if constexpr (S::xbar(Q)) bar();
    if constexpr (fn == 0 && rd) {
      bar();
      hook.template after_bar<QN / NF>();
    }
    if constexpr (n == 0) {
      wait_vm<C::behind_load((j + 1) * WM - 1, Q)>();
#pragma unroll
      for (int m = 0; m < WM; ++m) pin(s.x[u % XD][ks][m]);
    }
    SCHED();
    const bf16x8 w = cat8(s.lo[Q % WR], s.hi[Q % WR]);
#pragma unroll
    for (int m = 0; m < WM; ++m) {
      s.acc[m][n] = mfma(w, s.x[u % XD][ks][m], s.acc[m][n]);
      if constexpr (rd) {
        if (m == 0) { SCHED(); s.lo[QN % WR] = lds_tr(s.a[nn] + un * 16384 + ksn * 8192); SCHED(); }
        if (m == 1) {
          SCHED();
          s.hi[QN % WR] = lds_tr(s.a[nn] + un * 16384 + ksn * 8192 + 1024);
          if constexpr (un == 3 && ksn == 1) { s.a[nn] ^= 65536u; pin(s.a[nn]); }
          SCHED();
        }
      }
    }
    if constexpr (S::nld(Q) > 0) {
      static_assert((C::cum(Q + 1) - 1) / WM - 2 * XD < (Q + 1) / WN, "ring slot still in use");
      SCHED();
      dx_loads<S, C::cum(Q), S::nld(Q)>(s, xaddr, t0);
      SCHED();
    }
    dx_step<S, Q + 1>(s, xaddr, t0, hook);
  }
}
template <int WM, int WN, int XD, int WR, class RG> DI void dx_init(DX<WM, WN, XD, WR>& s, int lane, const RG& rowgrp, int ld8) {
  const u32 g = lane >> 4, q = (lane & 15) >> 2, p = lane & 3, xm = (q | ((g & 1) << 2)) << 1;
#pragma unroll
  for (int n = 0; n < WN; ++n) { s.a[n] = 65536u + (8 * g + q) * 256 + (((u32)(2 * n) + (p >> 1)) ^ xm) * 16u + (p & 1) * 8u; pin(s.a[n]); }
#pragma unroll
  for (int m = 0; m < WM; ++m) { s.vx[m] = (u32)(rowgrp(m) * ld8 * 256 + lane * 16); pin(s.vx[m]); }
}
template <int WM, int WN, int XD, int WR, class XA> DI void dx_first_x(DX<WM, WN, XD, WR>& s, const XA& xaddr) {
#pragma unroll
  for (int u = 0; u < XD; ++u) {
#pragma unroll
    for (int m = 0; m < WM; ++m) ldx<0>(s.x[u][0][m], xaddr(u), s.vx[m]);
    if (u < XD - 1) {
#pragma unroll
      for (int m = 0; m < WM; ++m) ldx<1024>(s.x[u][1][m], xaddr(u), s.vx[m]);
    }
  }
}
template <int WM, int WN, int XD, int WR> DI void dx_start(DX<WM, WN, XD, WR>& s) {
#pragma unroll
  for (int q = 0; q < WR - 1; ++q) { s.lo[q] = lds_tr(s.a[q % WN] + (q / WN) * 8192); s.hi[q] = lds_tr(s.a[q % WN] + (q / WN) * 8192 + 1024); }
#pragma unroll
  for (int m = 0; m < WM; ++m)
#pragma unroll
    for (int n = 0; n < WN; ++n) { s.acc[m][n] = (f32x4){0.f, 0.f, 0.f, 0.f}; pin(s.acc[m][n]); }
}
struct NoHook {
  template <int T> DI void after_bar() {}
  template <int I> DI void op() {}
};

template <int NPX> DI void x_voffs(int lane, int lw, int ld, u32 (&vx)[NPX]) {
#pragma unroll
  for (int j = 0; j < NPX; ++j) vx[j] = (u32)((8 * (lw * NPX + j) + (lane >> 3)) * ld) * 2u + (u32)(((lane & 7) ^ (lane >> 3)) << 4);
}
DI void w256_src(int lane, int piece, int& k, int& c) {
  k = 4 * piece + (lane >> 4);
  c = (lane & 15) ^ (((k & 3) | (((k >> 3) & 1) << 2)) << 1);
}
template <int WN_T> DI void give_rows(const f32x4 (&acc)[4][WN_T], f32x4 (&e)[2][WN_T], u32 slot) {
#pragma unroll
  for (int m = 0; m < 2; ++m)
#pragma unroll
    for (int n = 0; n < WN_T; ++n) { *(LAS f32x4*)(slot + (m * WN_T + n) * 1024) = acc[2 + m][n]; e[m][n] = acc[m][n]; }
}
template <int WN_T> DI void take_rows(f32x4 (&e)[2][WN_T], u32 slot) {
#pragma unroll
  for (int m = 0; m < 2; ++m)
#pragma unroll
    for (int n = 0; n < WN_T; ++n) e[m][n] = *(LAS f32x4*)(slot + (m * WN_T + n) * 1024);
}

DI bf16x8 ones8() { const u32 o = 0x3f803f80u; return __builtin_bit_cast(bf16x8, (u32x4){o, o, o, o}); }

constexpr u32 K1_DUMMY = 3 * 49152;
constexpr int K1_LDS = K1_DUMMY + 256;
constexpr u32 T1_GW = 131072, T1_ONE = T1_GW + 2048, T1_SUM = T1_ONE + 64, T1_SSQ = T1_SUM + 512, T1_CS = T1_SSQ + 512;

#define WW0 40
#define WW1 30
#define WT0 7
#define WTS 3
struct Warm {
  u32 wid, lds; bool on;
  const char *x, *wqkv, *w1;
  DI void go(const char* p) const {
    u32 keep;
    asm volatile("s_mov_b32 %0, m0\n\ts_mov_b32 m0, %1\n\ts_nop 3\n\tglobal_load_lds_dword %2, off\n\ts_mov_b32 m0, %0"
                 : "=&s"(keep) : "s"(lds), "v"(p) : "memory");
  }
  DI void first(int t0) const {
    if (!on) return;
    const u32 width = t0 ? WW1 : WW0, u0 = (t0 ? 2 * WW0 * 512 : 0) + wid * width + min((u32)fresh_lane(), width - 1);
#pragma nounroll
    for (int t = 0; t < 2; ++t) {
      const u32 u = min(u0 + t * width * 512, 14u * 5120u - 1u);
      const bool kv = u >= 14u * 3072u;
      const u32 k = 2 + (kv ? (u - 14u * 3072u) >> 11 : u / 3072), v = kv ? (u - 14u * 3072u) & 2047 : u % 3072;
      u32 o = v < 2048 ? v * 2048 + k * 128 : (k * 1024 + v - 2048) * 128;
      unsigned long long p = v < 2048 ? (unsigned long long)x : (unsigned long long)w1;
      pin(o); pin(p);
      o = kv ? (k * 64 + (v >> 5)) * 6144 + 2048 + (v & 31) * 128 : o;
      p = kv ? (unsigned long long)wqkv : p;
      pin(o); pin(p);
      go((const char*)(p + o));
    }
  }
};
template <int WM, int WN> struct StatExtra {
  static constexpr int N0 = WM == 0 ? 8 : 4, N1 = WM == 1 ? 8 : 4;
  f32x4 (&racc)[4]; f32x4 (&cacc)[4];
  u32 sa, sstep;
  bf16x8 a;
  const Warm& warm;
  DI void start() { if (WN == 1) a = lds_b128(sa); if constexpr (WM == 1) warm.first(2); }
  DI void pre_bar() {}
  template <int KS, int I, class F> DI void step(const F& f) {
    if constexpr (I < 4) {
      if constexpr (WN == 0) racc[I] = mfma(f.x[I], f.x[I], racc[I]);
      else {
        racc[I] = mfma(a, f.x[I], racc[I]);
        if constexpr (I == 3) { sa += sstep; a = lds_b128(sa); }
      }
    } else {
      cacc[I - 4] = mfma(f.w(I - 4), ones8(), cacc[I - 4]);
    }
  }
};
template <int WM, int WN>
DI void w1_loop(f32x4 (&acc)[4][4], f32x4 (&racc)[4], f32x4 (&cacc)[4], u32 sa, u32 sstep, int lane, char* ws, const Warm& warm) {
  StatExtra<WM, WN> ex{racc, cacc, sa, sstep, ones8(), warm};
  compute_loop<4, 4, 4, 32768, true, GA44, BI44, 2>(acc, 16, lane, WM * 64, 16384, WN * 4, ex);
}

constexpr u32 T3_CS = 131072, T3_RS = T3_CS + 512;
constexpr u32 K2_DUMMY = T3_RS + 4096, T4_G = K2_DUMMY + 256;
constexpr u32 T2_WO = K1_DUMMY, T2_KV = T4_G + 512, T2_Z = T2_KV + 2048, T2_G = T2_Z + 512;
constexpr int GM_LDS = T2_WO + 16384;
constexpr int K4_NT = KPG / 64 / 4;
#define W4H (xcd >> 2)
#define C_GQ 52
#define C_DQ 13
#define C_D 7
#define C_DW 72
#define C_WCAP 5
#define C_LEAD 12
struct CPlan {
  static constexpr int NT = K4_NT, D = C_D, NN = NT - D, NPH = 2, NU = 2 * NN + NPH * 2 * D, NS = NU * 8, NFR = NT * 8, L0 = 12, PRE = 12;
  static constexpr int ph_m(int p) { return p == 0 ? 0 : p + 2; }
  static constexpr int QL = (2 * NN + 2 * D) * 8;
  static constexpr int half(int t) { return ((t + 4) & 7) >> 2; }
  int ut[NU], uk[NU], um[NU], ux[NU + 1];
  int ft[NFR], fk[NFR], fm[NFR], fu[NFR];
  int seq[NS + 1], pos[NFR];
  int rd[NS + 1], cum[NS + 1];
  int bt[NS];
  constexpr CPlan() : ut{}, uk{}, um{}, ux{}, ft{}, fk{}, fm{}, fu{}, seq{}, pos{}, rd{}, cum{}, bt{} {
    int u = 0, f = 0, T[NS + 1] = {};
    auto add = [&](int t, int m0, int m1) {
      for (int k = 0; k < 2; ++k, ++u) {
        ut[u] = t; uk[u] = k; um[u] = m0; ux[u] = f;
        for (int m = m0; m < m1; ++m, ++f) { ft[f] = t; fk[f] = k; fm[f] = m; fu[f] = u; }
      }
    };
    for (int t = 0; t < NN; ++t) add(t, 0, 4);
    for (int p = 0; p < NPH; ++p) {
      const int h0 = half(ut[u - 1]);
      for (int pass = 0; pass < 2; ++pass)
        for (int t = NN; t < NT; ++t)
          if (p == 0 ? pass == 0 : (half(t) == h0) == (pass == 0)) add(t, ph_m(p), ph_m(p + 1));
    }
    ux[NU] = f;
    for (int q = 0; q < NS; ++q) { T[q + 1] = T[q] + ux[q / 8 + 1] - ux[q / 8]; bt[q] = -1; }
    rd[0] = 3; cum[0] = L0; seq[0] = L0 + PRE;
    for (int k = 0; k < L0; ++k) pos[k] = k;
    int want = L0;
    for (int q = 0; q < NS; ++q) {
      int r = rd[q];
      const int lead = q < 64 ? 12 : C_LEAD;
      while (r < NS && T[r] - T[q] <= lead && r - q <= (q < QL ? C_WCAP : C_WCAP + 2 * (q - QL))) {
        if (r % 16 == 0 && r < 16 * NT) bt[q] = ut[r / 8];
        ++r;
      }
      rd[q + 1] = r;
      if (q % 8 < ux[q / 8 + 1] - ux[q / 8] && want < NFR) ++want;
      int c = cum[q];
      const int rate = C_GQ > 52 ? (12 + 63 - C_GQ) / (64 - C_GQ) : 1;
      for (int k = 0; k < rate && c < want && (c < 32 || q >= C_GQ) && (c < 16 || fu[c - 16] * 8 + 7 <= q); ++k) ++c;
      for (int k = cum[q]; k < c; ++k) pos[k] = seq[q] + k - cum[q];
      seq[q + 1] = seq[q] + c - cum[q] + (q >= QL && q < QL + 12);
      cum[q + 1] = c;
    }
    if (bt[C_GQ] < 0) bt[C_GQ] = 100;
    if (bt[C_DQ] < 0) bt[C_DQ] = 100;
    bt[QL + C_DW] = 100;
  }
};
constexpr CPlan CP;
static_assert(CP.bt[13] == 1 && CP.bt[29] == 2 && CP.bt[45] == 3 && CP.bt[61] == 4 && CP.cum[CPlan::NS] == CPlan::NFR, "plan");
constexpr u32 HO_S = C_D == 8 ? 131072 : ((CPlan::NN + 3) & 7) * 16384;
struct CS {
  f32x4 acc[4][8];
  bf16x8 x[32];
  s16x4 lo[32], hi[32];
  u32 a[8], vx[4], o16;
};
template <int F, int N> DI void c_loads(CS& s, const char* xp, const char* xg) {
  if constexpr (N > 0) {
    ldx<CP.fk[F] * 1024>(s.x[F % 32], (CP.ft[F] < 4 ? xp : xg) + CP.ft[F] * 2048, s.vx[CP.fm[F]]);
    c_loads<F + 1, N - 1>(s, xp, xg);
  }
}
template <int R0, int J, int JE> DI void c_reads(CS& s) {
  if constexpr (J < JE) {
    constexpr int r = R0 + J / 2, u = r / 8, n = r % 8, off = (CP.ut[u] & 3) * 16384 + CP.uk[u] * 8192;
    if constexpr (J & 1) {
      s.hi[r % 32] = lds_tr(s.a[n] + off + 1024);
      if constexpr (u + 1 < CPlan::NU && CPlan::half(CP.ut[u]) != CPlan::half(CP.ut[u + 1 < CPlan::NU ? u + 1 : u])) { s.a[n] ^= 65536u; pin(s.a[n]); }
    } else {
      s.lo[r % 32] = lds_tr(s.a[n] + off);
    }
    c_reads<R0, J + 1, JE>(s);
  }
}
template <int Q, int I> DI void c_mfmas(CS& s, bf16x8 w) {
  constexpr int u = Q / 8, n = Q % 8, nm = CP.ux[u + 1] - CP.ux[u], nr = 2 * (CP.rd[Q + 1] - CP.rd[Q]), per = (nr + nm - 1) / nm;
  if constexpr (I < nm) {
    s.acc[CP.um[u] + I][n] = mfma(w, s.x[(CP.ux[u] + I) % 32], s.acc[CP.um[u] + I][n]);
    if constexpr (I * per < nr) { SCHED(); c_reads<CP.rd[Q], I * per, ((I + 1) * per < nr ? (I + 1) * per : nr)>(s); SCHED(); }
    c_mfmas<Q, I + 1>(s, w);
  }
}
template <int Q, int QE, class P> DI void c_step(CS& s, const char* xp, const char* xg, const P& part) {
  if constexpr (Q < QE) {
    constexpr int u = Q / 8, n = Q % 8, xl = CP.ux[u + 1] - 1;
    if constexpr (Q == C_DQ) wait_vm<CP.cum[Q] - CPlan::L0>();
    if constexpr (Q == CPlan::QL + C_DW) wait_vm<0>();
    if constexpr (CP.bt[Q] >= 0) bar();
    if constexpr (n == 0) {
      static_assert(CP.cum[Q] > xl, "not requested yet");
      static_assert(Q >= CPlan::QL + C_DW || CP.pos[xl] <= CP.seq[CPlan::QL], "would wait for the stores");
      if constexpr (Q < CPlan::QL + C_DW) wait_vm<CP.seq[Q] - 1 - CP.pos[xl]>();
#pragma unroll
      for (int f = CP.ux[u]; f <= xl; ++f) pin(s.x[f % 32]);
    }
    SCHED();
    c_mfmas<Q, 0>(s, cat8(s.lo[Q % 32], s.hi[Q % 32]));
    if constexpr (Q >= CPlan::NN * 16 && Q < CPlan::QL) pin(s.acc[3][n]);
    if constexpr (CP.cum[Q + 1] > CP.cum[Q]) {
      SCHED();
      c_loads<CP.cum[Q], CP.cum[Q + 1] - CP.cum[Q]>(s, xp, xg);
      SCHED();
    }
    if constexpr (Q >= CPlan::QL && Q < CPlan::QL + 12) {
      constexpr int i = Q - CPlan::QL, m = i % 3, hh = i / 3;
      st16c(part(m) + hh * 1024, s.o16, pack_pair(s.acc[m][2 * hh], s.acc[m][2 * hh + 1]));
      SCHED();
    }
    c_step<Q + 1, QE>(s, xp, xg, part);
  }
}

#define FHS 1
#define WSL 12

__global__ __launch_bounds__(512, 2) void gm_kernel(const u16* __restrict__ x, const u16* __restrict__ wqkv, const u16* __restrict__ wo,
                                                    const u16* __restrict__ w1, const u16* __restrict__ w2, const u16* __restrict__ w3,
                                                    const u16* __restrict__ w4, const u16* __restrict__ gw, char* __restrict__ ws,
                                                    u16* __restrict__ out) {
  extern __shared__ __attribute__((aligned(16))) char smem[];
  const int b = blockIdx.x, xcd = b & 7, idx = b >> 3;
  const int wave = __builtin_amdgcn_readfirstlane(threadIdx.x >> 6);
  int lane = threadIdx.x & 63, g = lane >> 4, l15 = lane & 15;
  if (wave < 4) {
    CODE_TOUCH(".Lgm_end4", K1_DUMMY, wave);
    if (idx < 4) CODE_TOUCH(".Lgm_end4", K1_DUMMY, 4 + idx * 4 + wave);
  }
  const int w4i = wave & 3, wm = w4i >> 1, wn = w4i & 1;
  const bool isq = idx >= 16;
  const int rp = (xcd >> 1) * 4 + ((idx >> 2) & 3), cp = (xcd & 1) * 4 + (idx & 3);
  const int rk = (xcd >> 1) * 2 + ((idx >> 3) & 1), hp = (xcd & 1) * 8 + (idx & 7);
  const char* const xb1 = (const char*)(x + (size_t)(isq ? rp * 128 : rk * 256) * DM);
  u32 vx1[8], vw1[4];
  auto voffs1 = [&]() {
    const u32 v0 = (u32)((8 * w4i * (isq ? 4 : 8) + (lane >> 3)) * DM) * 2u + (u32)(((lane & 7) ^ (lane >> 3)) << 4);
#pragma unroll
    for (int j = 0; j < 8; ++j) vx1[j] = v0 + j * (8 * DM * 2);
#pragma unroll
    for (int j = 0; j < 4; ++j) {
      int k, c; w256_src(lane, w4i * 4 + j, k, c);
      vw1[j] = (u32)(isq ? k * DM + cp * 128 + 8 * c : k * 3072 + 1024 * (1 + ((c >> 2) & 1)) + 32 * (2 * hp + (c >> 3)) + 8 * (c & 3)) * 2u;
    }
  };
  if (wave >= 4) {
    voffs1();
    if (isq && w4i < 2) glds16(T1_GW + w4i * 1024, (u32)(w4i * 1024 + lane * 16), gw);
#pragma nounroll
    for (int t = 0; t < (isq ? 4 : 3); ++t) {
      if (t == 2) {
        if (isq) wait_vm<8>(); else wait_vm<12>();
        bar();
      }
      const u32 sb = isq ? ((t + 2) & 3) * 32768 : t * 49152, bx = sb + w4i * (isq ? 4096 : 8192), bw = sb + (isq ? 16384 : 32768) + w4i * 4096;
      const char* wb_ = isq ? (const char*)w1 + t * (64 * DM * 2) : (const char*)wqkv + t * (64 * 3072 * 2);
#pragma unroll
      for (int j = 0; j < 4; ++j) glds16(bx + j * 1024, vx1[j], xb1 + t * 128);
      if (!isq) {
#pragma unroll
        for (int j = 4; j < 8; ++j) glds16(bx + j * 1024, vx1[j], xb1 + t * 128);
      }
#pragma unroll
      for (int j = 0; j < 4; ++j) glds16(bw + j * 1024, vw1[j], wb_);
    }
  }
  auto none = []() {};
  const int rp3 = (xcd >> 2) * 4 + (idx >> 3), cp3 = (xcd & 3) * 8 + (idx & 7);
  u32 vw3[4];
  bool val3[2];
  auto issue3w = [&](int t) {
    const char* wb = (const char*)w3 + (size_t)t * (64 * FF * 2);
    const u32 base = (u32)((t + 4) & 7) * 16384u + w4i * 4096u;
#pragma unroll
    for (int j = 0; j < 4; ++j) {
      if (val3[j >> 1]) glds16(base + j * 1024u, vw3[j], wb);
    }
  };
  {
    const int wb = xcd * 16 + (idx & 15);
    int r0, row, col;
    u32 slot;
    auto places = [&]() {
      lane = fresh_lane(); g = lane >> 4; l15 = lane & 15;
      r0 = isq ? wm * 64 + (wave >> 2) * 32 + l15 : w4i * 32 + (wave >> 2) * 16 + l15;
      row = rp * 128 + r0; col = cp * 128 + (isq ? wn * 64 : 0) + 16 * (g & 1) + 8 * (g >> 1);
      slot = (u32)(w4i * 8192 + lane * 16);
    };
    const int mh = wb >> 2, ms = wb & 3;
    LAS float* tsum = (LAS float*)T1_SUM; LAS float* tssq = (LAS float*)T1_SSQ; LAS float* tcs = (LAS float*)T1_CS;
    f32x4 e[2][4];
    u32x4 xr[4];
    auto ldxr = [&]() {
#pragma unroll
      for (int j = 0; j < 4; ++j) ld16(xr[j], x, (u32)(row * DM + col + j * 32) * 2u);
    };
    u32x4 v3[16];
    const int w3r = (wb / 28 & 3) * 4 + w4i, w3c = wb % 28 * 128;
    auto ld3 = [&](auto c) {
#pragma unroll
      for (int i = 4 * c.v; i < 4 * c.v + 4; ++i) ld16(v3[i], (const char*)w3 + 4 * i * FF * 2, (u32)((w3r * 64 + g) * FF + w3c + l15 * 8) * 2u);
    };
    auto sum3 = [&]() {
      float s[8];
#pragma unroll
      for (int q = 0; q < 8; ++q) s[q] = 0.f;
#pragma unroll
      for (int i = 0; i < 16; ++i) {
        pin(v3[i]);
#pragma unroll
        for (int q = 0; q < 4; ++q) { s[2 * q] += bf_lo(v3[i][q]); s[2 * q + 1] += bf_hi(v3[i][q]); }
      }
#pragma unroll
      for (int q = 0; q < 8; ++q) s[q] = grp_sum(s[q]);
      if (lane < 16 && wb < 112) {
        float* dst = (float*)(ws + OFF_CS3) + w3r * FF + w3c + lane * 8;
        stg16(dst, __builtin_bit_cast(u32x4, (f32x4){s[0], s[1], s[2], s[3]}));
        stg16(dst + 4, __builtin_bit_cast(u32x4, (f32x4){s[4], s[5], s[6], s[7]}));
      }
    };
    auto h_out = [&]() {
      f32x4 cs[4];
#pragma unroll
      for (int n = 0; n < 4; ++n) cs[n] = *(LAS f32x4*)(tcs + wn * 64 + n * 16 + 4 * g) + *(LAS f32x4*)(tcs + 128 + wn * 64 + n * 16 + 4 * g);
      const u32 o = (u32)((((row >> 4) + 1) * (DM / 8) + (col >> 3)) * 256 + l15 * 16);
#pragma unroll
      for (int m = 0; m < 2; ++m) {
        const float mean = tsum[r0 + m * 16] * (1.0f / DM);
        const float var = fmaxf(tssq[r0 + m * 16] * (1.0f / DM) - mean * mean, 0.0f);
        const float rstd = __builtin_amdgcn_rsqf(var + 1e-5f);
        const f32x4 k = gelu_consts(mean, rstd, (var + 1e-5f) * rstd);
#pragma unroll
        for (int n = 0; n < 4; ++n) e[m][n] = ln_gelu4(e[m][n], k, cs[n]);
        st16c(ws + OFF_H + m * 16 * DM * 2, o, pack_pair(e[m][0], e[m][1]));
        st16c(ws + OFF_H + m * 16 * DM * 2 + 1024, o, pack_pair(e[m][2], e[m][3]));
      }
    };
    if (wave >= 4) {
      const int lw = w4i;
      places();
      voffs1();
      Boot boot{ws, b, wave == 4 ? 0 : 2, 0u};
      if (wave == 4) boot.look();
      u32 vx[4], vw[4];
      x_voffs<4>(lane, lw, DM, vx);
      const int ldw = isq ? 3072 : DM;
#pragma unroll
      for (int j = 0; j < 4; ++j) {
        int k, c; w256_src(lane, lw * 4 + j, k, c);
        vw[j] = (u32)(k * ldw + cp * 128 + 8 * c) * 2u;
        vw3[j] = (u32)(k * FF + cp3 * 112 + 8 * c) * 2u;
        val3[j >> 1] = c < 14;
      }
      const u32 uw = (u32)((lw * 128 + wb) * 64 + lane);
      if (!isq) {
        {
          auto addr = [&](int t, const char*& xb, const char*& wb_) { xb = xb1 + t * 128; wb_ = (const char*)wqkv + (size_t)t * (64 * 3072 * 2); };
          BootMaster master{ws, (u32)lane * 16u, b == 0 && wave == 6 ? 0 : 2};
          if (master.state == 0) master.begin();
          auto each = [&](int t) {
            if (!(t & 1)) { boot.step(); master.step(); }
            if (t == WT0) { if (uw < 16384) touch(K1_DUMMY, (uw >> 4) * 6144 + (uw & 15) * 128, wqkv); else touch(K1_DUMMY, (uw - 16384) * 128, w2); }
            if (t == WT0 + WTS) { if (uw < 16384) touch(K1_DUMMY, uw * 128, wo); else touch(K1_DUMMY, (uw - 16384) * 128, w3); }
          };
          auto side = [&](auto c) { if (c.v == 1) { boot.finish(); master.finish(); } };
          loader_loop<8, 4, 3, 32768, 49152, 0, 0, false>(16, lw, vx1, vw1, addr, side, EachFn<4, decltype(each)>{each});
        }
        auto issue2w = [&](int t) {
          const char* wb_ = (const char*)w2 + (size_t)t * (64 * DM * 2);
          const u32 base = (u32)((t + 4) & 7) * 16384u + w4i * 4096u;
#pragma unroll
          for (int j = 0; j < 4; ++j) glds16(base + j * 1024u, vw[j], wb_);
        };
#pragma unroll
        for (int t = 0; t < 4; ++t) { issue2w(t); if (t == 0) ldxr(); }
        if (wave == 4) {
          const int l = min(lane, 23);
          wait_flags<FHS>((GAS u32*)(ws + OFF_FH) + min(max(rp - 1 + (l >> 3), 0), 15) * 64 + (l & 7));
        }
        wait_vm<16>();
        bar();
#pragma unroll
        for (int t = 4; t < 8; ++t) issue2w(t);
        wait_vm<24>();
        bar();
        if (wave == 5 && lane == 0) set_flag((GAS u32*)(ws + OFF_FKV) + wb);
#pragma unroll 1
        for (int t = 2; t < 10; ++t) {
          wait_vm<20>();
          bar();
          issue2w(t + 6);
        }
#pragma unroll
        for (int t = 10; t < 14; ++t) {
          wait_vm<20>();
          bar();
          issue3w(t - 10);
        }
        wait_vm<20>();
        bar();
        wait_vm<16>();
        bar();
      } else {
        const char* xb0 = (const char*)(x + (size_t)rp * 128 * DM);
        const char* wb0 = (const char*)wqkv;
        auto addr = [&](int t, const char*& xb, const char*& wb_) { xb = xb0 + t * 128; wb_ = wb0 + (size_t)t * (64 * 3072 * 2); };
        {
          auto addr1 = [&](int t, const char*& xb, const char*& wb_) { xb = xb0 + t * 128; wb_ = (const char*)w1 + (size_t)t * (64 * DM * 2); };
          auto side = [&](auto c) { if (c.v == 2) boot.finish(); };
          auto each = [&](int t) {
            if (t % 3 == 0) boot.step();
          };
          loader_loop<4, 4, 4, 16384, 32768, 0, 2, false>(16, lw, vx, vw1, addr1, side, EachFn<4, decltype(each)>{each});
          bar();
          take_rows<4>(e, slot);
          h_out();
          if ((rp == 0 || rp == 15) && lw == 0 && lane < 32)
            st16c(ws + OFF_H + (size_t)(rp ? SEQ / 16 + 1 : 0) * (DM * 32) + cp * 4096, (u32)((lane >> 1) * 256 + ((lane & 1) + (rp ? 0 : 14)) * 16), (u32x4){0u, 0u, 0u, 0u});
#pragma unroll
          for (int t = 0; t < 2; ++t) loader_issue<4, 4, 16384, 32768>(t, t + 2, lw, vx, vw, addr);
          wait_vm<16>();
          bar();
          if (wave == 5 && lane == 0) set_flag((GAS u32*)(ws + OFF_FH) + rp * 64 + cp);
#pragma unroll
          for (int t = 2; t < 4; ++t) loader_issue<4, 4, 16384, 32768>(t, t - 2, lw, vx, vw, addr);
          wait_vm<24>();
          bar();
        }
        f32x4 kp[16], zp[2];
        auto each = [&](int t) {
          if (t == 4 + lw || t == 9) touch(K2_DUMMY, min(uw + (t == 9 ? 49152u : 16384u), 57343u) * 128, w3);
          if (t == 12) asm volatile("global_load_dwordx2 v[248:249], %0, %1 sc1" ::"v"(lane * 8), "s"(ws + OFF_FKV) : "memory", "v248", "v249");
        };
        auto side = [&](auto c) {
          if (c.v == 0) {
            u32 f;
            asm volatile("v_and_b32 %0, v248, v249" : "=v"(f));
            if (!__all(f == 1u)) {
              wait_flags((GAS u32*)(ws + OFF_FKV) + lane);
              wait_flags((GAS u32*)(ws + OFF_FKV) + 64 + lane);
            }
#pragma unroll
            for (int i = 0; i < 2; ++i) ld16c(zp[i], ws + OFF_ZP + i * 32768, (u32)((lane >> 3) * 1024 + cp * 128 + lw * 32 + (lane & 7) * 4) * 4u);
#pragma unroll
            for (int p = 0; p < 16; ++p) ld16c(kp[p], ws + OFF_KVP + (size_t)(p * 32 + mh) * 4096, (u32)(lw * 64 + lane) * 16u);
          }
          ld3(c);
          if (c.v == 0) {
#pragma unroll
            for (int j = 0; j < 4; ++j) {
              int k, cc; w256_src(lane, lw * 2 + (j & 1), k, cc);
              glds16(T2_WO + (u32)((j >> 1) * 8 + lw * 2 + (j & 1)) * 1024u, (u32)((32 * mh + k) * DM + ms * 256 + (j >> 1) * 128 + 8 * cc) * 2u, wo);
            }
          } else if (c.v < 3) { issue3w(2 * c.v - 2); issue3w(2 * c.v - 1); }
        };
        loader_loop<4, 4, 4, 16384, 32768, 121226, 2, false>(16, lw, vx, vw, addr, side, EachFn<4, decltype(each)>{each});
        side(IC<3>{});
        wait_vm<28>();
        pin(zp[0]); pin(zp[1]);
        zp[0] += zp[1];
#pragma unroll
        for (int r = 0; r < 4; ++r) zp[0][r] = grp_sum(ror_add<8>(zp[0][r]));
        if (lane < 8) *(LAS f32x4*)(T2_Z + (lw * 32 + lane * 4) * 4) = zp[0];
#pragma unroll
        for (int p = 0; p < 16; ++p) pin(kp[p]);
#pragma unroll
        for (int p = 0; p < 8; ++p) kp[p] += kp[p + 8];
        const f32x4 sk = ((kp[0] + kp[1]) + (kp[2] + kp[3])) + ((kp[4] + kp[5]) + (kp[6] + kp[7]));
        *(LAS u32x2*)(T2_KV + (lw * 64 + lane) * 8) = (u32x2){pk2(sk[0], sk[1]), pk2(sk[2], sk[3])};
      }
      wait_lgkm0();
      bar();
      take_rows<4>(e, slot);
    } else {
      const Warm warm{(u32)(b * 2 + (wave & 1)), isq ? K2_DUMMY : K1_DUMMY, wave >= 2, (const char*)x, (const char*)wqkv, (const char*)w1};
      if (wave >= 2) __builtin_amdgcn_s_sleep(WSL);
      warm.first(0);
      auto warm23 = [&]() { warm.first(2); };
      if (!isq) {
        f32x4 acc[8][4];
        places();
        EXTRA(0, warm23);
        compute_loop<8, 4, 3, 49152, false, GA84, BI84>(acc, 16, lane, wm * 128, 32768, wn * 4, ex);
        places();
        f32x4 kv[2][2], z[2];
#pragma unroll
        for (int i = 0; i < 2; ++i) { z[i] = (f32x4){0.f, 0.f, 0.f, 0.f}; kv[0][i] = z[i]; kv[1][i] = z[i]; }
        const bf16x8 one = ones8();
#pragma unroll
        for (int mp = 0; mp < 4; ++mp) {
          bf16x8 pkf[2], vf[2];
#pragma unroll
          for (int i = 0; i < 2; ++i) {
            const f32x4 a = acc[2 * mp][i], c = acc[2 * mp + 1][i], d = acc[2 * mp][2 + i], e_ = acc[2 * mp + 1][2 + i];
            pkf[i] = __builtin_bit_cast(bf16x8, (u32x4){pk2(phi(a[0]), phi(a[1])), pk2(phi(a[2]), phi(a[3])), pk2(phi(c[0]), phi(c[1])), pk2(phi(c[2]), phi(c[3]))});
            vf[i] = __builtin_bit_cast(bf16x8, (u32x4){pk2(d[0], d[1]), pk2(d[2], d[3]), pk2(e_[0], e_[1]), pk2(e_[2], e_[3])});
          }
#pragma unroll
          for (int i = 0; i < 2; ++i) {
            kv[0][i] = mfma(vf[0], pkf[i], kv[0][i]);
            kv[1][i] = mfma(vf[1], pkf[i], kv[1][i]);
            z[i] = mfma(one, pkf[i], z[i]);
          }
        }
        const int ph = (rk * 2 + wm) * 32 + 2 * hp + wn;
        asm volatile("s_nop 7\n\ts_nop 7\n\ts_nop 3" : "+v"(kv[0][0]), "+v"(kv[0][1]), "+v"(kv[1][0]), "+v"(kv[1][1]));
#pragma unroll
        for (int j = 0; j < 2; ++j)
#pragma unroll
          for (int i = 0; i < 2; ++i) st16c(ws + OFF_KVP + (size_t)ph * 4096 + i * 2048 + j * 64, (u32)(l15 * 128 + g * 16), __builtin_bit_cast(u32x4, kv[j][i]));
        if (g < 2) stg4(ws + OFF_ZP + ph * 128 + lane * 4, __float_as_uint(g ? z[1][0] : z[0][0]));
        ldxr();
        DX<2, 8, 4, 8> s;
        NoHook hook;
        const char* hb = ws + OFF_H + (size_t)rp * (128 * DM * 2);
        auto xaddr = [&](int t) { return hb + t * 2048; };
        auto shifted = [&](int q, int m) {
          const int r = 16 + wave * 32 + m * 16 + l15 + (q & 2 ? 2 : 1) * (q & 1 ? 1 : -1);
          return (u32)((r >> 4) * (DM * 32) + (r & 15) * 16 + g * 256);
        };
        dx_init(s, lane, [&](int) { return 0; }, 0);
#pragma unroll
        for (int i = 0; i < 4; ++i) { s.vx[i] = shifted(i >> 1, i & 1); pin(s.vx[i]); }
        bar();
        dx_first_x(s, xaddr);
        dx_start(s);
#pragma unroll 1
        for (int t0 = 0; t0 < 12; t0 += 4) {
          dx_step<Plain<2, 8, 4, 4, false>, 0>(s, xaddr, t0, hook);
#pragma unroll
          for (int m = 0; m < 2; ++m) { s.vx[m] = s.vx[2 + m]; s.vx[2 + m] = shifted((t0 >> 2) + 2, m); pin(s.vx[m]); pin(s.vx[2 + m]); }
        }
        dx_step<Plain<2, 8, 4, 4, true>, 0>(s, xaddr, 12, hook);
#pragma unroll
        for (int i = 0; i < 8; ++i) { *(LAS f32x4*)(slot + i * 1024) = s.acc[1][i]; e[i >> 2][i & 3] = s.acc[0][i]; }
      } else {
        places();
        if (wave == 0 && lane == 0) *(LAS u32x4*)T1_ONE = (u32x4){0x3f803f80u, 0x3f803f80u, 0x3f803f80u, 0x3f803f80u};
        f32x4 acc[4][4], racc[4], cacc[4];
#pragma unroll
        for (int m = 0; m < 4; ++m) { racc[m] = (f32x4){0.f, 0.f, 0.f, 0.f}; cacc[m] = racc[m]; }
        const u32 sa = (l15 == 1) ? T1_GW + g * 16 : T1_ONE, sstep = (l15 == 1) ? 64u : 0u;
        if (wave == 0) w1_loop<0, 0>(acc, racc, cacc, sa, sstep, lane, ws, warm);
        else if (wave == 1) w1_loop<0, 1>(acc, racc, cacc, sa, sstep, lane, ws, warm);
        else if (wave == 2) w1_loop<1, 0>(acc, racc, cacc, sa, sstep, lane, ws, warm);
        else w1_loop<1, 1>(acc, racc, cacc, sa, sstep, lane, ws, warm);
        if (wn == 0) {
          if ((l15 >> 2) == g) {
#pragma unroll
            for (int m = 0; m < 4; ++m) {
              const f32x4 v = racc[m];
              const int r = lane & 3;
              tssq[wm * 64 + m * 16 + l15] = r == 0 ? v[0] : r == 1 ? v[1] : r == 2 ? v[2] : v[3];
            }
          }
        } else if (g == 0) {
#pragma unroll
          for (int m = 0; m < 4; ++m) {
            tsum[wm * 64 + m * 16 + l15] = racc[m][0];
            const float gate = __builtin_amdgcn_rcpf(1.0f + __builtin_amdgcn_exp2f(-1.4426950408889634f * racc[m][1]));
            *(LAS float*)(T4_G + (wm * 64 + m * 16 + l15) * 4) = gate;
            if (cp == 0) stg4(ws + OFF_G + (rp * 128 + wm * 64 + m * 16 + l15) * 4, __float_as_uint(gate));
          }
        }
        if (l15 == 0) {
#pragma unroll
          for (int n = 0; n < 4; ++n) *(LAS f32x4*)(tcs + wm * 128 + wn * 64 + n * 16 + 4 * g) = cacc[n];
        }
        give_rows<4>(acc, e, slot);
        wait_lgkm0();
        bar();
        h_out();
        wait_vm<0>();
        bar();
        EXTRA(1, none);
        compute_loop<4, 4, 4, 32768, true, GA44, BI44, 2>(acc, 16, lane, wm * 64, 16384, wn * 4, ex);
        give_rows<4>(acc, e, slot);
      }
      wait_lgkm0();
      bar();
    }
    if (isq) {
      f32x4 zt[4];
#pragma unroll
      for (int n = 0; n < 4; ++n) zt[n] = *(LAS f32x4*)(T2_Z + (wn * 64 + n * 16 + 4 * g) * 4);
      char* pg = ws + OFF_PG + ((size_t)(row >> 4) * (KPG / 8) + (col >> 3)) * 256 + l15 * 16;
#pragma unroll
      for (int m = 0; m < 2; ++m) {
#pragma unroll
        for (int hh = 0; hh < 2; ++hh) {
          f32x4 a = e[m][2 * hh], c = e[m][2 * hh + 1];
          float den = 0.f;
#pragma unroll
          for (int r = 0; r < 4; ++r) { a[r] = phi(a[r]); c[r] = phi(c[r]); den += a[r] * zt[2 * hh][r] + c[r] * zt[2 * hh + 1][r]; }
          den = grp_sum(den) + 1e-6f;
          const float sc = *(LAS float*)(T4_G + (r0 + m * 16) * 4) / den;
          stg16(pg + (m * (KPG / 8) + 4 * hh) * 256, pack_pair(a * sc, c * sc));
        }
      }
      if (wave >= 4) {
        wait_vm<4>();
        sum3();
      } else {
        const u32 q = l15 >> 2, p = lane & 3, xm = (q | ((g & 1) << 2)) << 1;
        bf16x8 wf[4];
#pragma unroll
        for (int n = 0; n < 4; ++n) {
          const u32 a = T2_WO + wm * 8192 + (8 * g + q) * 256 + (((u32)(2 * (4 * wn + n)) + (p >> 1)) ^ xm) * 16u + (p & 1) * 8u;
          wf[n] = cat8(lds_tr(a), lds_tr(a + 1024));
        }
        u16* mp = (u16*)(ws + OFF_M) + (size_t)(32 * mh + l15) * DM + ms * 256 + wave * 64 + 16 * (g & 1) + 8 * (g >> 1);
#pragma unroll
        for (int i = 0; i < 2; ++i) {
          const bf16x8 kf = lds_b128(T2_KV + (16 * i + l15) * 64 + g * 16);
          f32x4 mo[4];
#pragma unroll
          for (int n = 0; n < 4; ++n) mo[n] = mfma(wf[n], kf, (f32x4){0.f, 0.f, 0.f, 0.f});
          stg16(mp + (size_t)16 * i * DM, pack_pair(mo[0], mo[1]));
          stg16(mp + (size_t)16 * i * DM + 32, pack_pair(mo[2], mo[3]));
        }
      }
    } else {
      char* x1 = ws + OFF_X1 + ((size_t)(row >> 4) * (DM / 8) + (col >> 3)) * 256 + l15 * 16;
      char* st = ws + OFF_ST + ((size_t)(row >> 6) * 8 + cp) * 1024 + (row & 63) * 16;
#pragma unroll
      for (int m = 0; m < 2; ++m) {
        float v[16];
#pragma unroll
        for (int hh = 0; hh < 2; ++hh) {
          f32x4 a = e[m][2 * hh], c = e[m][2 * hh + 1];
          swap_pair_f32(a, c);
          pin(xr[2 * m + hh]);
          const u32x4 o = axpy_pack(a, c, 1.0f, xr[2 * m + hh]);
          stg16(x1 + (m * 8 + 4 * hh) * 256, o);
#pragma unroll
          for (int q = 0; q < 4; ++q) { v[8 * hh + 2 * q] = bf_lo(o[q]); v[8 * hh + 2 * q + 1] = bf_hi(o[q]); }
        }
        float s = 0.f;
#pragma unroll
        for (int q = 0; q < 16; ++q) s += v[q];
        const float mean = grp_sum(s) * (1.0f / 64.0f);
        float q2 = 0.f;
#pragma unroll
        for (int q = 0; q < 16; ++q) q2 += (v[q] - mean) * (v[q] - mean);
        q2 = grp_sum(q2);
        if (g == 0) stg8(st + m * 8, (u32x2){__float_as_uint(mean), __float_as_uint(q2)});
      }
    }
    wait_vm<0>();
    wait_lgkm0();
    bar();
    if (wave == 5 && lane == 0) set_flag(isq ? (GAS u32*)(ws + OFF_FQ) + rp * 8 + cp : (GAS u32*)(ws + OFF_FX) + (rp >> 1) * 32 + (rp & 1) * 8 + cp);
  }
  const int rq = rp3, cq = idx & 7, kq = xcd & 3, tile = rq * 8 + cq;
  GAS u32* const flag = (GAS u32*)(ws + OFF_FLG + tile * 128);
  const int hh0 = (wave >> 2) * 2;
  u32x4 xr[2];
  float gr;
  auto last_ld = [&]() {
    const int lane = fresh_lane(), g = lane >> 4, l15 = lane & 15;
    const u32 o = (u32)(((rq * 16 + kq * 4 + w4i) * (DM / 8) + cq * 16 + 4 * hh0 + 2 * (g & 1) + (g >> 1)) * 256 + l15 * 16);
    ld16c(xr[0], ws + OFF_X1, o);
    ld16c(xr[1], ws + OFF_X1, o + 1024u);
    ld4fc(gr, ws + OFF_G, (u32)(rq * 256 + kq * 64 + w4i * 16 + l15) * 4u);
  };
  auto last_step = [&](f32x4 (&own)[2][2], auto who) {
    const int lane = fresh_lane(), g = lane >> 4, l15 = lane & 15;
    const char* part = ws + OFF_PRT + (size_t)((tile * 4 + kq) * 3) * 16384;
    const u32 o = (u32)((w4i * 4 + hh0) * 1024 + lane * 16);
    u32x4 p[3][2];
#pragma unroll
    for (int j = 0; j < 3; ++j) { ld16c(p[j][0], part + j * 16384, o); ld16c(p[j][1], part + j * 16384, o + 1024u); }
    if constexpr (who.v == 1) {
#pragma unroll
      for (int i = 0; i < 4; ++i) own[i >> 1][i & 1] = *(LAS f32x4*)(HO_S + w4i * 4096 + i * 1024 + lane * 16);
    }
    wait_vm<6>();
    pin(gr); pin(xr[0]); pin(xr[1]);
    u16* dst = out + (size_t)(rq * 256 + kq * 64 + w4i * 16 + l15) * DM + cq * 128 + 32 * hh0 + 16 * (g & 1) + 8 * (g >> 1);
#pragma unroll
    for (int i = 0; i < 2; ++i)
#pragma unroll
      for (int q = 0; q < 4; ++q) {
        own[i][q >> 1][2 * (q & 1)] += (1.0f - gr) * bf_lo(xr[i][q]);
        own[i][q >> 1][2 * (q & 1) + 1] += (1.0f - gr) * bf_hi(xr[i][q]);
      }
    wait_vm<0>();
#pragma unroll
    for (int i = 0; i < 2; ++i) {
      pin(p[0][i]); pin(p[1][i]); pin(p[2][i]);
#pragma unroll
      for (int q = 0; q < 4; ++q) {
        own[i][q >> 1][2 * (q & 1)] += bf_lo(p[0][i][q]) + bf_lo(p[1][i][q]);
        own[i][q >> 1][2 * (q & 1) + 1] += bf_hi(p[0][i][q]) + bf_hi(p[1][i][q]);
      }
      st16w(dst + 32 * i, axpy_pack(own[i][0], own[i][1], 1.0f, p[2][i]));
    }
    asm volatile("; end %0" ::"n"(who.v));
  };
  const u32 slot = (u32)(w4i * 4096 + lane * 16);
  auto spos = [](int i) { return (u32)((i >> 2) * 16384 + (i & 3) * 1024); };
  char* const pg = ws + OFF_PG + ((size_t)(rp3 * 16 + w4i * 4) * (KPG / 8) + DM / 8 + cp3 * 14) * 256;
  auto finish = [&](f32x4 (&e)[7], int m, int lane) {
    const int g = lane >> 4, l15 = lane & 15;
    const u32 o16 = (u32)((2 * (g & 1) + (g >> 1)) * 256 + l15 * 16), o8 = (u32)((g >> 1) * 256 + (g & 1) * 8 + l15 * 16);
    const f32x4 k = *(LAS f32x4*)(T3_RS + (w4i * 64 + m * 16 + l15) * 16);
#pragma unroll
    for (int n = 0; n < 7; ++n) {
      e[n] = ln_gelu4(e[n], k, *(LAS f32x4*)(T3_CS + (n * 16 + 4 * g) * 4));
      if (n & 1) st16c(pg + (m * (KPG / 8) + 2 * (n - 1)) * 256, o16, pack_pair(e[n - 1], e[n]));
    }
    st8c(pg + (m * (KPG / 8) + 12) * 256, o8, (u32x2){pk2(e[6][0], e[6][1]), pk2(e[6][2], e[6][3])});
  };
  if (wave >= 4) {
    const int lw = w4i, rp = rp3, cp = cp3;
    auto barT = [&]() { bar(); };
    auto pass = [&]() { wait_lgkm0(); barT(); };
    const int lane = fresh_lane(), g = lane >> 4, l15 = lane & 15;
    const u32 slot = (u32)(w4i * 4096 + lane * 16);
    u32 vw4[4];
#pragma unroll
    for (int j = 0; j < 4; ++j) {
      int k, c; w256_src(lane, lw * 4 + j, k, c);
      vw3[j] = (u32)(k * FF + cp3 * 112 + 8 * c) * 2u;
      val3[j >> 1] = c < 14;
      vw4[j] = (u32)(k * DM + cq * 128 + 8 * c) * 2u;
      pin(vw4[j]);
    }
    u32 ocs = (u32)(g * FF + cp * 112 + min(lw * 8 + (l15 & 7), 27) * 4) * 4u, o16 = (u32)lane * 16u, ow4 = (u32)(kq * 14336 + W4H * 7168 + (idx * 4 + lw) * 56 + min(lane, 55)) * 128u;
    pin(ocs); pin(o16); pin(ow4);
#pragma unroll
    for (int t = 4; t < 8; ++t) issue3w(t);
    if (wave == 4) wait_flags((GAS u32*)(ws + OFF_FX) + rp * 32 + l15);
    barT();
    barT();
    u32x2 fq;
    f32x4 sv[8], cs[4];
    float gt;
    const char* stb = ws + OFF_ST + (rp * 4 + lw) * 8192;
    ld8c(fq, ws + OFF_FQ, o16 >> 1);
    ld4fc(gt, ws + OFF_G + (rp * 256 + lw * 64) * 4, o16 >> 2);
#pragma unroll
    for (int i = 0; i < 4; ++i) ld16c(sv[i], stb + i * 1024, o16);
    barT();
    issue3w(8);
#pragma unroll
    for (int i = 4; i < 8; ++i) ld16c(sv[i], stb + i * 1024, o16);
    barT();
    issue3w(9);
    wait_vm<30>();
    barT();
    issue3w(10);
    wait_vm<30>();
    barT();
    issue3w(11);
    wait_vm<12>();
    float mean = 0.f, m2 = 0.f;
#pragma unroll
    for (int i = 0; i < 8; ++i) { pin(sv[i]); mean += sv[i][0] + sv[i][2]; m2 += sv[i][1] + sv[i][3]; }
    mean *= 1.0f / 16.0f;
    pin(mean); pin(m2);
    barT();
    issue3w(12);
    {
      float dd = 0.f;
#pragma unroll
      for (int i = 0; i < 8; ++i) { const float d0 = sv[i][0] - mean, d1 = sv[i][2] - mean; dd += d0 * d0; dd += d1 * d1; }
      const float rs = __builtin_amdgcn_rsqf((m2 + 64.0f * dd) * (1.0f / DM) + 1e-5f);
      pin(gt);
      *(LAS f32x4*)(T3_RS + lw * 1024 + o16) = gelu_consts(mean, rs, __builtin_amdgcn_rcpf(fmaxf(rs * (1.0f - gt), 1e-30f)));
    }
    barT();
    issue3w(13);
    pin(fq);
    if (!__all(fq[0] == 1u && fq[1] == 1u)) {
      wait_flags((GAS u32*)(ws + OFF_FQ) + lane);
      wait_flags((GAS u32*)(ws + OFF_FQ) + 64 + lane);
    }
    u32x4 mr[4][4];
    auto ldm = [&](int t) {
#pragma unroll
      for (int j = 0; j < 4; ++j) ld16c(mr[t & 3][j], ws + OFF_M + (size_t)(4 * kq + t) * (64 * DM * 2), vw4[j]);
    };
    auto wrm = [&](int t) {
#pragma unroll
      for (int j = 0; j < 4; ++j) { pin(mr[t & 3][j]); *(LAS u32x4*)(slot + ((t + 4) & 7) * 16384 + j * 1024) = mr[t & 3][j]; }
    };
#pragma unroll
    for (int i = 0; i < 4; ++i) ld16c(cs[i], ws + OFF_CS3 + i * (4 * FF * 4), ocs);
    wait_vm<24>();
    barT();
    issue3w(14);
    ldm(0);
    wait_vm<28>();
    barT();
    issue3w(15);
    ldm(1);
    wait_vm<32>();
#pragma unroll
    for (int t = 0; t < 4; ++t) {
      barT();
      if (t == 0) wait_vm<8>(); else if (t == 1) wait_vm<4>(); else if (t == 2) wait_vm<5>(); else wait_vm<1>();
      wrm(t);
      if (t < 2) ldm(t + 2);
      if (t == 1) touch(K2_DUMMY, ow4, w4);
      if (t == 2) {
        pin(cs[0]); pin(cs[1]); pin(cs[2]); pin(cs[3]);
        cs[0] = (cs[0] + cs[1]) + (cs[2] + cs[3]);
#pragma unroll
        for (int r = 0; r < 4; ++r) cs[0][r] = grp_sum(cs[0][r]);
        if (lane < 8) *(LAS f32x4*)(T3_CS + lw * 128 + o16) = cs[0];
      }
    }
    barT();
    barT();
    pass();
    f32x4 e[7];
#pragma unroll
    for (int i = 0; i < 7; ++i) e[i] = *(LAS f32x4*)(slot + spos(i));
    finish(e, 3, lane);
    GAS u32* fg = (GAS u32*)(ws + OFF_FG) + rp * 32;
    const char* w44 = (const char*)w4 + (size_t)(14 * kq - 4) * (64 * DM * 2);
    auto dmaw = [&](int t) {
      const char* wb = w44 + (size_t)t * (64 * DM * 2);
      const u32 base = (u32)((t + 4) & 7) * 16384u + w4i * 4096u;
#pragma unroll
      for (int j = 0; j < 4; ++j) glds16(base + j * 1024u, vw4[j], wb);
    };
    u32 have = 0;
    auto need_g = [&](int t) {
      if (wave != 4) return;
      const u32 need = (2u << ((4 * (t - 4) + 3) / 7)) - 1u;
      int spins = 0;
      while ((have & need) != need && ++spins < (1 << 22)) {
        have = (u32)__ballot(__hip_atomic_load(fg + kq * 8 + (lane & 7), __ATOMIC_RELAXED, __HIP_MEMORY_SCOPE_AGENT) == 1u);
        if ((have & need) != need) __builtin_amdgcn_s_sleep(1);
      }
    };
#pragma unroll
    for (int t = 4; t < 8; ++t) dmaw(t);
    barT();
#pragma unroll
    for (int q = 0; q < 64; ++q) {
      if (q != C_GQ && q != C_DQ && q % 16 != 13) continue;
      if (q == C_DQ) wait_vm<16 + (C_DQ > 29 ? 4 : 0)>();
      if (q == C_GQ) need_g(5);
      if (q == 61) { need_g(6); wait_vm<20>(); }
      barT();
      if (q == C_DQ && wave == 5 && lane == 0) set_flag(fg + cp);
      if (q % 16 == 13 && q > 13) dmaw(q / 16 + 7);
    }
#pragma unroll 1
    for (int t = 5; t <= K4_NT - 7; ++t) {
      need_g(t < CPlan::NN - 2 ? t + 2 : K4_NT - 1);
      wait_vm<20>();
      barT();
      dmaw(t + 6);
    }
    wait_vm<20>();
    barT();
    wait_vm<16>();
    barT();
    wait_vm<12>();
    barT();
    wait_vm<8>();
    barT();
    wait_vm<4>();
    barT();
    wait_vm<0>();
    barT();
    last_ld();
    barT();
    if (wave == 4) {
      if (lane == 0) set_flag(flag + kq);
      wait_flags(flag + (lane & 3));
    }
    barT();
    f32x4 own[2][2];
    last_step(own, IC<1>{});
    return;
  }
  CS c;
  const char* xb4 = ws + OFF_PG + (size_t)rq * 256 * KPG * 2;
  const char* xp4 = xb4 + kq * 4 * 2048;
  const char* xg4 = xb4 + (12 + kq * 14) * 2048;
  NoHook hook;
  {
    DX<4, 7, 2, 4> s;
    const char* xb3 = ws + OFF_X1 + (size_t)rp3 * 256 * DM * 2;
    auto xaddr3 = [&](int t) { return xb3 + t * 2048; };
    dx_init(s, lane, [&](int m) { return wave * 4 + m; }, DM / 8);
    bar();
    dx_first_x(s, xaddr3);
    dx_start(s);
#pragma unroll 1
    for (int t0 = 0; t0 < 12; t0 += 4) dx_step<Plain<4, 7, 2, 4, false>, 0>(s, xaddr3, t0, hook);
    dx_step<Plain<4, 7, 2, 4, true>, 0>(s, xaddr3, 12, hook);
#pragma unroll
    for (int i = 0; i < 7; ++i) *(LAS f32x4*)(slot + spos(i)) = s.acc[3][i];
    wait_lgkm0();
    bar();
    {
      const u32 q = l15 >> 2, p = lane & 3, xm = (q | ((g & 1) << 2)) << 1;
#pragma unroll
      for (int n = 0; n < 8; ++n) { c.a[n] = 65536u + (8 * g + q) * 256 + (((u32)(2 * n) + (p >> 1)) ^ xm) * 16u + (p & 1) * 8u; pin(c.a[n]); }
#pragma unroll
      for (int m = 0; m < 4; ++m) { c.vx[m] = (u32)((((kq + 1 + m) & 3) * 4 + wave) * (KPG / 8) * 256 + lane * 16); pin(c.vx[m]); }
    }
    c_loads<0, CPlan::L0>(c, xp4, xg4);
#pragma unroll
    for (int m = 0; m < 3; ++m) finish(s.acc[m], m, lane);
  }
  {
    bar();
    c_reads<0, 0, 6>(c);
#pragma unroll
    for (int m = 0; m < 4; ++m)
#pragma unroll
      for (int n = 0; n < 8; ++n) { c.acc[m][n] = (f32x4){0.f, 0.f, 0.f, 0.f}; pin(c.acc[m][n]); }
    auto part = [&](int m) { return ws + OFF_PRT + (size_t)((tile * 4 + ((kq + 1 + m) & 3)) * 3 + m) * 16384; };
    c_step<0, 160>(c, xp4, xg4, part);
    c_step<160, CPlan::QL>(c, xp4, xg4, part);
    c.o16 = (u32)(wave * 4096 + fresh_lane() * 16);
    c_step<CPlan::QL, CPlan::QL + C_DW + 1>(c, xp4, xg4, part);
    last_ld();
    c_step<CPlan::QL + C_DW + 1, CPlan::NS>(c, xp4, xg4, part);
#pragma unroll
    for (int hh = 0; hh < 4; ++hh) swap_pair_f32(c.acc[3][2 * hh], c.acc[3][2 * hh + 1]);
#pragma unroll
    for (int i = 0; i < 4; ++i) *(LAS f32x4*)(HO_S + c.o16 + i * 1024) = c.acc[3][4 + i];
    f32x4 own[2][2] = {{c.acc[3][0], c.acc[3][1]}, {c.acc[3][2], c.acc[3][3]}};
    wait_lgkm0();
    bar();
    last_step(own, IC<0>{});
  }
}

__global__ __attribute__((used)) void code_end_kernel() { CODE_MARK(".Lgm_end4"); }

}

extern "C" void kernel_launch(void* const* d_in, const int* in_sizes, int n_in, void* d_out, int out_size, void* d_ws, size_t ws_size,
                              hipStream_t stream) {
  const u16* x = (const u16*)d_in[0];
  const u16* wqkv = (const u16*)d_in[1];
  const u16* wo = (const u16*)d_in[2];
  const u16* w1 = (const u16*)d_in[4];
  const u16* w2 = (const u16*)d_in[5];
  const u16* w3 = (const u16*)d_in[6];
  const u16* w4 = (const u16*)d_in[7];
  const u16* gw = (const u16*)d_in[8];
  char* ws = (char*)d_ws;
  (void)hipFuncSetAttribute((const void*)gm_kernel, hipFuncAttributeMaxDynamicSharedMemorySize, GM_LDS);
  gm_kernel<<<256, 512, GM_LDS, stream>>>(x, wqkv, wo, w1, w2, w3, w4, gw, ws, (u16*)d_out);
}
```
